```python
import torch

def _softmax(x, axis=-1):
    return torch.softmax(x, dim=axis)
def _gelu(x):
    return torch.nn.functional.gelu(x, approximate="tanh")
def _silu(x):
    return torch.nn.functional.silu(x)
def _sigmoid(x):
    return torch.sigmoid(x)
def _layernorm(x, axis=-1, eps=1e-5):
    m = x.mean(dim=axis, keepdim=True)
    v = x.var(dim=axis, keepdim=True, unbiased=False)
    return (x - m) / torch.sqrt(v + eps)
def _scatter_add(tgt, idx, src, axis=0):
    return tgt.clone().index_add_(axis, idx, src)
def _segment_sum(src, seg, n):
    out = torch.zeros((n,) + src.shape[1:], dtype=src.dtype)
    return out.index_add_(0, seg, src)
def _pad(x, pw):
    flat = tuple(v for pair in reversed(pw) for v in pair)
    return torch.nn.functional.pad(x, flat)

kernel_name = 'standalone__cross_network_dcn__recsys'

def setup_inputs():

    _w = (lambda a: (lambda t: t.to(torch.bfloat16) if t.is_floating_point() else t)(torch.as_tensor(a)))
    B, F, D, L = 16384, 26, 16, 6
    Din = min(F * D, 1024)
    return {
        "x0": _w((lambda *s: torch.randn(*s, dtype=torch.bfloat16) * 0.05)(B, Din)),
        "ws": _w((lambda *s: torch.randn(*s, dtype=torch.bfloat16) * 0.8)(L, Din, Din) * (Din ** -0.5)),
        "bs": _w((lambda *s: torch.randn(*s, dtype=torch.bfloat16) * 3.2)(L, Din) * 0.01),
    }

def reference(x0, ws, bs):
    x = x0
    L = ws.shape[0]
    for l in range(L):
        proj = torch.einsum('bd,de->be', x, ws[l]) + bs[l]
        x = x0 * proj + x
    return x
```

```cpp
#include <hip/hip_runtime.h>
#include <hip/hip_bf16.h>

namespace {
constexpr int kD = 416, kL = 6, kNFR = 85;
constexpr unsigned kStream = 522240u, kFlagOff = 2088960u, kFlag2Off = 2121856u, kZOff = 2121728u, kWsBytes = 2125952u;
typedef unsigned v4u __attribute__((ext_vector_type(4)));
constexpr int kNt = 2, kSc1 = 16;

struct Unit { unsigned src, gs, cs, rs, d0, d1, d2, zf; };
__device__ __forceinline__ Unit make_unit(unsigned layer, unsigned sg, unsigned w, bool sh) {
  Unit u;
  const bool k12 = sg == 12u, nat = k12 || layer == 0u;
  const unsigned w1 = sg / 3u, j1 = sg - 3u * w1;
  u.src = ((layer * kD + (nat ? 32u * sg : 96u * w1 + 2u * j1)) * kD + (sh ? 384u : 96u * w)) * 2u;
  u.gs = (nat ? 8u : 24u) * kD * 2u;
  u.cs = sh ? 4u : 12u;
  u.rs = (nat ? 2u : 6u) * kD * 2u;
  const unsigned par = k12 ? 0u : (layer == 0u ? sg & 1u : w1 & 1u), wv = sh ? par : w;
  const unsigned t0 = sg + 12u - 3u * wv, s0 = k12 ? 12u : (t0 >= 12u ? t0 - 12u : t0), s1 = k12 ? 12u : (s0 >= 6u ? s0 - 6u : s0 + 6u);
  const unsigned o = (unsigned)(0x8c76b54a32910ull >> (4u * s0)) & 15u;
  const unsigned a0 = layer == 0u ? sg >> 1 : (unsigned)(0x4000632000510ull >> (4u * s0)) & 15u, a1 = layer == 0u ? sg >> 1 : (unsigned)(0x4000632000510ull >> (4u * s1)) & 15u;
  const unsigned base = (wv * kL + layer) * kNFR * 1024u;
  u.d0 = base + (sh ? 16u + a0 + (a0 >= 4u ? 10u : 0u) : (o < 8u ? 0u : 4u) + 2u * o) * 1024u;
  u.d1 = base + (sh ? 2u * kL * kNFR + 16u + a1 + (a1 >= 4u ? 10u : 0u) : 33u + 2u * o) * 1024u;
  u.d2 = base + (59u + 2u * o) * 1024u;
  u.zf = sh && k12;
  return u;
}

__device__ __forceinline__ float bf2f(unsigned short b) { return __builtin_bit_cast(float, (unsigned)b << 16); }
__device__ __forceinline__ unsigned short f2bf(float f) {
  unsigned u = __builtin_bit_cast(unsigned, f);
  u += 0x7fffu + ((u >> 16) & 1u);
  return (unsigned short)(u >> 16);
}
__device__ __forceinline__ void slow_path(const unsigned short* x0, const unsigned short* w, const unsigned short* bs, unsigned short* out,
                                          unsigned short* xs, unsigned blk, unsigned t) {
  for (unsigned half = 0; half < 2u; ++half) {
    const unsigned short* xr = x0 + (size_t)(blk * 64u + half * 32u) * kD;
    unsigned short* xa = xs;
    unsigned short* xb = xs + 32 * kD;
    __syncthreads();
    for (unsigned i = t; i < 32u * kD; i += 256u) xa[i] = xr[i];
    __syncthreads();
    for (int l = 0; l < kL; ++l) {
      for (unsigned n = t; n < (unsigned)kD; n += 256u) {
        float acc[32];
#pragma unroll
        for (int r = 0; r < 32; ++r) acc[r] = 0.f;
        for (int k = 0; k < kD; ++k) {
          const float wv = bf2f(w[((size_t)l * kD + k) * kD + n]);
#pragma unroll
          for (int r = 0; r < 32; ++r) acc[r] += bf2f(xa[r * kD + k]) * wv;
        }
        const float b = bf2f(bs[l * kD + n]);
#pragma unroll
        for (int r = 0; r < 32; ++r) xb[r * kD + n] = f2bf(bf2f(xr[r * kD + n]) * (acc[r] + b) + bf2f(xa[r * kD + n]));
      }
      __syncthreads();
      unsigned short* tmp = xa; xa = xb; xb = tmp;
    }
    unsigned short* orow = out + (size_t)(blk * 64u + half * 32u) * kD;
    for (unsigned i = t; i < 32u * kD; i += 256u) orow[i] = xa[i];
  }
}

__global__ void __launch_bounds__(256, 1) dcn_main(const void* x0, const void* w, void* wp, const void* bs, void* out) {
  constexpr unsigned xbytes = 16384u * kD * 2u;
  __shared__ __attribute__((aligned(16))) char smem[135424];

  unsigned tid = threadIdx.x;
  {
    unsigned long long pc;
    unsigned off = tid * 128u;
    asm volatile("s_getpc_b64 %0\n\t.Ldcn_hot_begin:\n\tv_min_u32_e32 %1, .Ldcn_hot_end-.Ldcn_hot_begin, %1\n\tglobal_load_dword v250, %1, %0"
                 : "=&s"(pc), "+v"(off) :: "v250", "memory");
  }
  const unsigned wv = __builtin_amdgcn_readfirstlane(tid >> 6), blk = blockIdx.x;
  const unsigned lds = (unsigned)(size_t)(__attribute__((address_space(3))) char*)smem;
  const __amdgpu_buffer_rsrc_t sX = __builtin_amdgcn_make_buffer_rsrc((void*)x0, 0, xbytes, 0x00020000);
  const __amdgpu_buffer_rsrc_t sO = __builtin_amdgcn_make_buffer_rsrc(out, 0, xbytes, 0x00020000);
  const __amdgpu_buffer_rsrc_t sW = __builtin_amdgcn_make_buffer_rsrc(wp, 0, kWsBytes, 0x00020000);
  const __amdgpu_buffer_rsrc_t sB = __builtin_amdgcn_make_buffer_rsrc((void*)bs, 0, (unsigned)(kL * kD * 2), 0x00020000);
  unsigned tag, stat;

  const __amdgpu_buffer_rsrc_t sS = __builtin_amdgcn_make_buffer_rsrc((void*)w, 0, (unsigned)(kL * kD * kD * 2), 0x00020000);
  Unit ua = {}, ub = {};
  if (wv == 3u) {
    ua = make_unit(1u + blk / 52u, (blk % 52u) >> 2, blk & 3u, false);
    const bool sh = blk < 78u;
    const unsigned e = blk - 78u, j = e >> 2;
    ub = make_unit(sh ? 0u + blk / 13u : 5u, sh ? blk % 13u : 12u, e & 3u, sh);
    if (!sh) { ub.src += 4u * j; ub.d0 = j == 0u ? ub.d0 : (j == 1u ? ub.d1 : ub.d2); ub.d1 = ub.d0 + 1024u; }
    if (blk >= 78u + 12u) { ub.src = 0x7ff00000u; ub.d0 = ub.d1 = 0x7ff00000u; }
  }
  const unsigned bz = __builtin_amdgcn_readfirstlane(ub.zf);
  asm volatile(
    "v_and_b32_e32 v16, 63, v0\n\t"
    "s_and_b32 s44, %[wv], 1\n\t"
    "v_lshrrev_b32_e32 v14, 4, v16\n\t"
    "v_and_b32_e32 v13, 15, v16\n\t"
    "s_mul_i32 s42, %[wv], 192\n\t"
    "v_mul_u32_u24_e32 v252, 6656, v14\n\t"
    "s_mul_i32 s52, s44, 26624\n\t"
    "v_mad_u32_u24 v252, v13, 12, v252\n\t"
    "v_add_u32_e32 v252, s42, v252\n\t"
    "v_add_u32_e32 v253, 3328, v252\n\t"
    "v_mul_u32_u24_e32 v108, 6656, v14\n\t"
    "v_lshl_add_u32 v108, v13, 2, v108\n\t"
    "v_add_u32_e32 v109, 3328, v108\n\t"
    "s_cmp_lg_u32 %[wv], 3\n\ts_cbranch_scc1 88f\n\ts_mov_b32 s42, 2121728\n\tbuffer_load_dword v65, off, %[sW], s42 sc1\n\ts_add_u32 s55, 0, s52\n\tbuffer_load_dwordx3 v[192:194], v252, %[sS], s55 offen offset:0\n\tbuffer_load_dwordx3 v[196:198], v252, %[sS], s55 offen offset:832\n\tbuffer_load_dwordx3 v[200:202], v252, %[sS], s55 offen offset:1664\n\tbuffer_load_dwordx3 v[204:206], v252, %[sS], s55 offen offset:2496\n\tbuffer_load_dwordx3 v[208:210], v253, %[sS], s55 offen offset:0\n\tbuffer_load_dwordx3 v[212:214], v253, %[sS], s55 offen offset:832\n\tbuffer_load_dwordx3 v[216:218], v253, %[sS], s55 offen offset:1664\n\tbuffer_load_dwordx3 v[220:222], v253, %[sS], s55 offen offset:2496\n\ts_sub_u32 s55, 26624, s52\n\tbuffer_load_dwordx3 v[128:130], v252, %[sS], s55 offen offset:0\n\tbuffer_load_dwordx3 v[132:134], v252, %[sS], s55 offen offset:832\n\tbuffer_load_dwordx3 v[136:138], v252, %[sS], s55 offen offset:1664\n\tbuffer_load_dwordx3 v[140:142], v252, %[sS], s55 offen offset:2496\n\tbuffer_load_dwordx3 v[144:146], v253, %[sS], s55 offen offset:0\n\tbuffer_load_dwordx3 v[148:150], v253, %[sS], s55 offen offset:832\n\tbuffer_load_dwordx3 v[152:154], v253, %[sS], s55 offen offset:1664\n\tbuffer_load_dwordx3 v[156:158], v253, %[sS], s55 offen offset:2496\n\t88:\n\t"
    "s_lshr_b32 s42, %[blk], 3\n\t"
    "s_lshl_b32 s42, s42, 2\n\t"
    "s_add_u32 s42, s42, %[wv]\n\t"
    "s_mul_i32 s42, s42, 2816\n\t"
    "v_min_u32_e32 v32, 21, v16\n\t"
    "v_lshlrev_b32_e32 v32, 7, v32\n\t"
    "buffer_load_dword v110, v32, %[sS], s42 offen\n\t"
    "s_mul_i32 s46, %[blk], 53248\n\t"
    "s_lshr_b32 s43, %[wv], 1\n\t"
    "s_add_u32 s47, s46, 13312\n\t"
    "s_add_u32 s48, s46, 26624\n\t"
    "s_add_u32 s49, s46, 39936\n\t"
    "s_movk_i32 s50, 832\n\t"
    "s_mul_i32 s42, %[wv], 192\n\t"
    "v_mov_b32_e32 v41, s42\n\t"
    "v_mov_b32_e32 v42, 576\n\t"
    "v_cmp_gt_u32_e32 vcc, 48, v16\n\t"
    "s_cmp_eq_u32 %[wv], 3\n\t"
    "v_mul_u32_u24_e32 v40, 171, v16\n\t"
    "v_cndmask_b32_e32 v43, v42, v41, vcc\n\t"
    "v_lshl_add_u32 v43, v16, 2, v43\n\t"
    "buffer_load_dword v20, v43, %[sB], 0 offen offset:0\n\t"
    "buffer_load_dword v21, v43, %[sB], 0 offen offset:832\n\t"
    "buffer_load_dword v22, v43, %[sB], 0 offen offset:1664\n\t"
    "buffer_load_dword v23, v43, %[sB], 0 offen offset:2496\n\t"
    "buffer_load_dword v24, v43, %[sB], 0 offen offset:3328\n\t"
    "buffer_load_dword v25, v43, %[sB], s50 offen offset:3328\n\t"
    "s_cbranch_scc1 90f\n\t"
    "v_bfe_u32 v32, v16, 2, 3\n\t"
    "v_and_b32_e32 v33, 3, v16\n\t"
    "v_lshrrev_b32_e32 v34, 5, v16\n\t"
    "v_mul_u32_u24_e32 v35, 1664, v32\n\t"
    "v_lshl_add_u32 v35, v33, 4, v35\n\t"
    "v_lshl_add_u32 v35, v34, 6, v35\n\t"
    "s_lshl_b32 s42, %[wv], 8\n\t"
    "v_add_u32_e32 v36, s42, v35\n\t"
    "s_mul_i32 s42, %[wv], 13312\n\t"
    "s_add_u32 s45, s46, s42\n\t"
    "buffer_load_dwordx4 a[0:3], v36, %[sX], s46 offen offset:0\n\t"
    "buffer_load_dwordx4 a[4:7], v36, %[sX], s46 offen offset:128\n\t"
    "buffer_load_dwordx4 a[8:11], v36, %[sX], s46 offen offset:896\n\t"
    "buffer_load_dwordx4 a[12:15], v36, %[sX], s46 offen offset:1024\n\t"
    "buffer_load_dwordx4 a[16:19], v36, %[sX], s47 offen offset:0\n\t"
    "buffer_load_dwordx4 a[20:23], v36, %[sX], s47 offen offset:128\n\t"
    "buffer_load_dwordx4 a[24:27], v36, %[sX], s47 offen offset:896\n\t"
    "buffer_load_dwordx4 a[28:31], v36, %[sX], s47 offen offset:1024\n\t"
    "buffer_load_dwordx4 a[32:35], v36, %[sX], s48 offen offset:0\n\t"
    "buffer_load_dwordx4 a[36:39], v36, %[sX], s48 offen offset:128\n\t"
    "buffer_load_dwordx4 a[40:43], v36, %[sX], s48 offen offset:896\n\t"
    "buffer_load_dwordx4 a[44:47], v36, %[sX], s48 offen offset:1024\n\t"
    "buffer_load_dwordx4 a[48:51], v36, %[sX], s49 offen offset:0\n\t"
    "buffer_load_dwordx4 a[52:55], v36, %[sX], s49 offen offset:128\n\t"
    "buffer_load_dwordx4 a[56:59], v36, %[sX], s49 offen offset:896\n\t"
    "buffer_load_dwordx4 a[60:63], v36, %[sX], s49 offen offset:1024\n\t"
    "buffer_load_dwordx4 a[64:67], v35, %[sX], s45 offen offset:768\n\t"
    "buffer_load_dwordx4 a[68:71], v35, %[sX], s49 offen offset:768\n\t"
    "90:\n\t"
    "v_lshlrev_b32_e32 v15, 4, v16\n\t"
    "s_mul_i32 s42, %[wv], 522240\n\t"
    "v_add_u32_e32 v1, s42, v15\n\t"
    "v_and_b32_e32 v46, 1, v14\n\t"
    "v_lshlrev_b32_e32 v46, 4, v46\n\t"
    "v_and_b32_e32 v47, 2, v14\n\t"
    "v_lshl_or_b32 v46, v47, 5, v46\n\t"
    "v_xor_b32_e32 v6, v15, v46\n\t"
    "v_add_u32_e32 v6, %[lds], v6\n\t"
    "s_add_u32 s42, %[wv], 0\n\t"
    "s_and_b32 s42, s42, 3\n\t"
    "s_mul_i32 s42, s42, 12288\n\t"
    "v_add_u32_e32 v2, s42, v6\n\t"
    "s_add_u32 s42, %[wv], 1\n\t"
    "s_and_b32 s42, s42, 3\n\t"
    "s_mul_i32 s42, s42, 12288\n\t"
    "v_add_u32_e32 v3, s42, v6\n\t"
    "s_add_u32 s42, %[wv], 2\n\t"
    "s_and_b32 s42, s42, 3\n\t"
    "s_mul_i32 s42, s42, 12288\n\t"
    "v_add_u32_e32 v4, s42, v6\n\t"
    "s_add_u32 s42, %[wv], 3\n\t"
    "s_and_b32 s42, s42, 3\n\t"
    "s_mul_i32 s42, s42, 12288\n\t"
    "v_add_u32_e32 v5, s42, v6\n\t"
    "s_lshl_b32 s42, %[wv], 12\n\t"
    "s_add_u32 s42, s42, 53248\n\t"
    "v_add_u32_e32 v7, s42, v6\n\t"
    "s_lshl_b32 s45, %[wv], 10\n\t"
    "s_add_u32 s42, s45, 53248\n\t"
    "v_add_u32_e32 v8, s42, v6\n\t"
    "s_add_u32 s42, s45, 49152\n\t"
    "v_add_u32_e32 v9, s42, v6\n\t"
    "s_lshl_b32 s42, %[wv], 9\n\t"
    "s_add_u32 s42, s42, 69632\n\t"
    "s_add_u32 s42, s42, %[lds]\n\t"
    "v_lshl_add_u32 v10, v14, 4, s42\n\t"
    "v_lshrrev_b32_e32 v40, 9, v40\n\t"
    "v_mul_u32_u24_e32 v41, 3, v40\n\t"
    "v_sub_u32_e32 v41, v16, v41\n\t"
    "v_lshlrev_b32_e32 v40, 2, v40\n\t"
    "v_lshl_add_u32 v40, v41, 7, v40\n\t"
    "v_lshlrev_b32_e32 v41, 2, v16\n\t"
    "v_add_u32_e32 v41, 192, v41\n\t"
    "v_cndmask_b32_e32 v40, v41, v40, vcc\n\t"
    "v_add_u32_e32 v40, s42, v40\n\t"
    "s_lshl_b32 s42, s44, 12\n\t"
    "v_add_u32_e32 v114, 82176, v6\n\t"
    "v_add_u32_e32 v112, s42, v114\n\t"
    "v_subrev_u32_e32 v113, s42, v114\n\t"
    "s_lshr_b32 s42, %[blk], 3\n\t"
    "s_lshl_b32 s42, s42, 2\n\t"
    "s_add_u32 s42, s42, %[wv]\n\t"
    "s_lshl_b32 s42, s42, 3\n\t"
    "v_add_u32_e32 v50, s42, v16\n\t"
    "v_and_b32_e32 v51, 3, v50\n\t"
    "v_lshrrev_b32_e32 v50, 2, v50\n\t"
    "v_lshlrev_b32_e32 v50, 7, v50\n\t"
    "s_mov_b32 s42, 522240\n\t"
    "v_mul_lo_u32 v51, v51, s42\n\t"
    "v_add_u32_e32 v50, v50, v51\n\t"
    "v_mov_b32_e32 v51, 0x7ff00000\n\t"
    "v_cmp_gt_u32_e32 vcc, 8, v16\n\t"
    "s_nop 1\n\t"
    "v_cndmask_b32_e32 v11, v51, v50, vcc\n\t"
    "s_mul_i32 s42, %[wv], 12288\n\t"
    "s_add_u32 s42, s42, %[lds]\n\t"
    "s_add_u32 s42, s42, 82176\n\t"
    "v_lshrrev_b32_e32 v52, 4, v16\n\t"
    "v_mul_u32_u24_e32 v52, 3, v52\n\t"
    "v_and_b32_e32 v53, 15, v16\n\t"
    "v_lshlrev_b32_e32 v53, 4, v53\n\t"
    "v_and_b32_e32 v54, 1, v52\n\t"
    "v_lshlrev_b32_e32 v54, 4, v54\n\t"
    "v_and_b32_e32 v55, 2, v52\n\t"
    "v_lshl_or_b32 v54, v55, 5, v54\n\t"
    "v_xor_b32_e32 v54, v54, v53\n\t"
    "v_and_b32_e32 v55, 3, v52\n\t"
    "v_lshl_add_u32 v54, v55, 8, v54\n\t"
    "v_lshrrev_b32_e32 v55, 2, v52\n\t"
    "v_lshl_add_u32 v54, v55, 12, v54\n\t"
    "v_add_u32_e32 v0, s42, v54\n\t"
    "v_add_u32_e32 v52, 1, v52\n\t"
    "v_and_b32_e32 v54, 1, v52\n\t"
    "v_lshlrev_b32_e32 v54, 4, v54\n\t"
    "v_and_b32_e32 v55, 2, v52\n\t"
    "v_lshl_or_b32 v54, v55, 5, v54\n\t"
    "v_xor_b32_e32 v54, v54, v53\n\t"
    "v_and_b32_e32 v55, 3, v52\n\t"
    "v_lshl_add_u32 v54, v55, 8, v54\n\t"
    "v_lshrrev_b32_e32 v55, 2, v52\n\t"
    "v_lshl_add_u32 v54, v55, 12, v54\n\t"
    "v_add_u32_e32 v254, s42, v54\n\t"
    "v_add_u32_e32 v52, 1, v52\n\t"
    "v_and_b32_e32 v54, 1, v52\n\t"
    "v_lshlrev_b32_e32 v54, 4, v54\n\t"
    "v_and_b32_e32 v55, 2, v52\n\t"
    "v_lshl_or_b32 v54, v55, 5, v54\n\t"
    "v_xor_b32_e32 v54, v54, v53\n\t"
    "v_and_b32_e32 v55, 3, v52\n\t"
    "v_lshl_add_u32 v54, v55, 8, v54\n\t"
    "v_lshrrev_b32_e32 v55, 2, v52\n\t"
    "v_lshl_add_u32 v54, v55, 12, v54\n\t"
    "v_add_u32_e32 v255, s42, v54\n\t"
    "s_mov_b32 s53, 0x05040100\n\t"
    "s_mov_b32 s54, 0x07060302\n\t"
    "s_cmp_eq_u32 %[wv], 3\n\ts_cbranch_scc1 91f\n\t"
    "v_and_b32_e32 v46, 1, v33\n\t"
    "v_lshlrev_b32_e32 v46, 4, v46\n\t"
    "v_and_b32_e32 v47, 2, v33\n\t"
    "v_lshl_or_b32 v46, v47, 5, v46\n\t"
    "s_add_u32 s55, %[lds], 82176\n\t"
    "v_lshlrev_b32_e32 v56, 5, v32\n\t"
    "v_xor_b32_e32 v56, v56, v46\n\t"
    "v_lshl_add_u32 v56, v33, 8, v56\n\t"
    "v_xor_b32_e32 v57, 16, v56\n\t"
    "v_add_u32_e32 v56, s55, v56\n\t"
    "v_add_u32_e32 v57, s55, v57\n\t"
    "v_add_u32_e32 v59, 49152, v56\n\t"
    "v_cmp_eq_u32_e32 vcc, 0, v34\n\t"
    "s_lshl_b32 s42, %[wv], 10\n\t"
    "s_lshl_b32 s45, %[wv], 14\n\t"
    "v_cndmask_b32_e32 v59, v57, v59, vcc\n\t"
    "v_add_u32_e32 v58, s42, v59\n\t"
    "v_lshl_add_u32 v56, v34, 12, v56\n\t"
    "v_lshl_add_u32 v57, v34, 12, v57\n\t"
    "v_add_u32_e32 v56, s45, v56\n\t"
    "s_add_u32 s45, s45, 4096\n\t"
    "v_add_u32_e32 v57, s45, v57\n\t"
    "v_mov_b32_e32 v28, 1.0\n\t"
    "v_mov_b32_e32 v29, 1.0\n\t"
    "s_waitcnt vmcnt(23)\n\t"
    "v_lshlrev_b32_e32 v26, 16, v20\n\t"
    "v_and_b32_e32 v27, 0xffff0000, v20\n\t"
    "v_add_f32_e32 v30, v28, v26\n\t"
    "v_add_f32_e32 v31, v29, v27\n\t"
    "ds_write_b32 v40, v30 offset:0\n\t"
    "ds_write_b32 v40, v31 offset:64\n\t"
    "s_waitcnt vmcnt(22)\n\t"
    "v_lshlrev_b32_e32 v26, 16, v21\n\t"
    "v_and_b32_e32 v27, 0xffff0000, v21\n\t"
    "v_add_f32_e32 v28, v30, v26\n\t"
    "v_add_f32_e32 v29, v31, v27\n\t"
    "ds_write_b32 v40, v28 offset:2048\n\t"
    "ds_write_b32 v40, v29 offset:2112\n\t"
    "s_waitcnt vmcnt(21)\n\t"
    "v_lshlrev_b32_e32 v26, 16, v22\n\t"
    "v_and_b32_e32 v27, 0xffff0000, v22\n\t"
    "v_add_f32_e32 v30, v28, v26\n\t"
    "v_add_f32_e32 v31, v29, v27\n\t"
    "ds_write_b32 v40, v30 offset:4096\n\t"
    "ds_write_b32 v40, v31 offset:4160\n\t"
    "s_waitcnt vmcnt(20)\n\t"
    "v_lshlrev_b32_e32 v26, 16, v23\n\t"
    "v_and_b32_e32 v27, 0xffff0000, v23\n\t"
    "v_add_f32_e32 v28, v30, v26\n\t"
    "v_add_f32_e32 v29, v31, v27\n\t"
    "ds_write_b32 v40, v28 offset:6144\n\t"
    "ds_write_b32 v40, v29 offset:6208\n\t"
    "s_waitcnt vmcnt(19)\n\t"
    "v_lshlrev_b32_e32 v26, 16, v24\n\t"
    "v_and_b32_e32 v27, 0xffff0000, v24\n\t"
    "v_add_f32_e32 v30, v28, v26\n\t"
    "v_add_f32_e32 v31, v29, v27\n\t"
    "ds_write_b32 v40, v30 offset:8192\n\t"
    "ds_write_b32 v40, v31 offset:8256\n\t"
    "s_waitcnt vmcnt(18)\n\t"
    "v_lshlrev_b32_e32 v26, 16, v25\n\t"
    "v_and_b32_e32 v27, 0xffff0000, v25\n\t"
    "v_add_f32_e32 v28, v30, v26\n\t"
    "v_add_f32_e32 v29, v31, v27\n\t"
    "ds_write_b32 v40, v28 offset:10240\n\t"
    "ds_write_b32 v40, v29 offset:10304\n\t"
    "s_waitcnt vmcnt(17)\n\t"
    "ds_write_b128 v56, a[0:3] offset:0\n\t"
    "s_waitcnt vmcnt(16)\n\t"
    "ds_write_b128 v56, a[4:7] offset:8192\n\t"
    "s_waitcnt vmcnt(15)\n\t"
    "ds_write_b128 v57, a[8:11] offset:0\n\t"
    "s_waitcnt vmcnt(14)\n\t"
    "ds_write_b128 v57, a[12:15] offset:8192\n\t"
    "s_waitcnt vmcnt(13)\n\t"
    "ds_write_b128 v56, a[16:19] offset:1024\n\t"
    "s_waitcnt vmcnt(12)\n\t"
    "ds_write_b128 v56, a[20:23] offset:9216\n\t"
    "s_waitcnt vmcnt(11)\n\t"
    "ds_write_b128 v57, a[24:27] offset:1024\n\t"
    "s_waitcnt vmcnt(10)\n\t"
    "ds_write_b128 v57, a[28:31] offset:9216\n\t"
    "s_waitcnt vmcnt(9)\n\t"
    "ds_write_b128 v56, a[32:35] offset:2048\n\t"
    "s_waitcnt vmcnt(8)\n\t"
    "ds_write_b128 v56, a[36:39] offset:10240\n\t"
    "s_waitcnt vmcnt(7)\n\t"
    "ds_write_b128 v57, a[40:43] offset:2048\n\t"
    "s_waitcnt vmcnt(6)\n\t"
    "ds_write_b128 v57, a[44:47] offset:10240\n\t"
    "s_waitcnt vmcnt(5)\n\t"
    "ds_write_b128 v56, a[48:51] offset:3072\n\t"
    "s_add_u32 s55, 0, s52\n\t"
    "buffer_load_dwordx3 v[192:194], v252, %[sS], s55 offen offset:0\n\t"
    "buffer_load_dwordx3 v[196:198], v252, %[sS], s55 offen offset:832\n\t"
    "buffer_load_dwordx3 v[200:202], v252, %[sS], s55 offen offset:1664\n\t"
    "s_waitcnt vmcnt(7)\n\t"
    "ds_write_b128 v56, a[52:55] offset:11264\n\t"
    "buffer_load_dwordx3 v[204:206], v252, %[sS], s55 offen offset:2496\n\t"
    "buffer_load_dwordx3 v[208:210], v253, %[sS], s55 offen offset:0\n\t"
    "buffer_load_dwordx3 v[212:214], v253, %[sS], s55 offen offset:832\n\t"
    "s_waitcnt vmcnt(9)\n\t"
    "ds_write_b128 v57, a[56:59] offset:3072\n\t"
    "buffer_load_dwordx3 v[216:218], v253, %[sS], s55 offen offset:1664\n\t"
    "buffer_load_dwordx3 v[220:222], v253, %[sS], s55 offen offset:2496\n\t"
    "s_sub_u32 s55, 26624, s52\n\t"
    "buffer_load_dwordx3 v[128:130], v252, %[sS], s55 offen offset:0\n\t"
    "s_waitcnt vmcnt(11)\n\t"
    "ds_write_b128 v57, a[60:63] offset:11264\n\t"
    "buffer_load_dwordx3 v[132:134], v252, %[sS], s55 offen offset:832\n\t"
    "buffer_load_dwordx3 v[136:138], v252, %[sS], s55 offen offset:1664\n\t"
    "buffer_load_dwordx3 v[140:142], v252, %[sS], s55 offen offset:2496\n\t"
    "s_waitcnt vmcnt(13)\n\t"
    "ds_write_b128 v58, a[64:67] offset:0\n\t"
    "buffer_load_dwordx3 v[144:146], v253, %[sS], s55 offen offset:0\n\t"
    "buffer_load_dwordx3 v[148:150], v253, %[sS], s55 offen offset:832\n\t"
    "buffer_load_dwordx3 v[152:154], v253, %[sS], s55 offen offset:1664\n\t"
    "s_waitcnt vmcnt(15)\n\t"
    "ds_write_b128 v59, a[68:71] offset:3072\n\t"
    "buffer_load_dwordx3 v[156:158], v253, %[sS], s55 offen offset:2496\n\t"
    "s_branch 94f\n\t"
    "91:\n\tv_mul_lo_u32 v66, v14, %[bG]\n\tv_mul_lo_u32 v67, v13, %[bC]\n\tv_add_u32_e32 v66, v66, v67\n\ts_mov_b32 s42, %[bS]\n\tbuffer_load_dword a240, v66, %[sS], s42 offen offset:0 nt\n\tbuffer_load_dword a241, v66, %[sS], s42 offen offset:832 nt\n\ts_add_u32 s42, s42, %[bR]\n\tbuffer_load_dword a242, v66, %[sS], s42 offen offset:0 nt\n\tbuffer_load_dword a243, v66, %[sS], s42 offen offset:832 nt\n\ts_add_u32 s42, s42, %[bR]\n\tbuffer_load_dword a244, v66, %[sS], s42 offen offset:0 nt\n\tbuffer_load_dword a245, v66, %[sS], s42 offen offset:832 nt\n\ts_add_u32 s42, s42, %[bR]\n\tbuffer_load_dword a246, v66, %[sS], s42 offen offset:0 nt\n\tbuffer_load_dword a247, v66, %[sS], s42 offen offset:832 nt\n\tv_mul_lo_u32 v67, v14, %[aG]\n\tv_mad_u32_u24 v67, v13, 12, v67\n\ts_mov_b32 s42, %[aS]\n\tbuffer_load_dwordx3 a[208:210], v67, %[sS], s42 offen offset:0 nt\n\tbuffer_load_dwordx3 a[212:214], v67, %[sS], s42 offen offset:832 nt\n\ts_add_u32 s42, s42, %[aR]\n\tbuffer_load_dwordx3 a[216:218], v67, %[sS], s42 offen offset:0 nt\n\tbuffer_load_dwordx3 a[220:222], v67, %[sS], s42 offen offset:832 nt\n\ts_add_u32 s42, s42, %[aR]\n\tbuffer_load_dwordx3 a[224:226], v67, %[sS], s42 offen offset:0 nt\n\tbuffer_load_dwordx3 a[228:230], v67, %[sS], s42 offen offset:832 nt\n\ts_add_u32 s42, s42, %[aR]\n\tbuffer_load_dwordx3 a[232:234], v67, %[sS], s42 offen offset:0 nt\n\tbuffer_load_dwordx3 a[236:238], v67, %[sS], s42 offen offset:832 nt\n\ts_waitcnt vmcnt(16)\n\tv_readfirstlane_b32 s51, v65\n\ts_nop 0\n\ts_add_u32 s51, s51, 1\n\tv_mov_b32_e32 v62, 0\n\tv_mov_b32_e32 v63, s51\n\ts_add_u32 s42, %[lds], 81920\n\tv_mov_b32_e32 v64, s42\n\tds_write_b64 v64, v[62:63]\n\tv_mov_b32_e32 v28, 1.0\n\tv_mov_b32_e32 v29, 1.0\n\tv_lshlrev_b32_e32 v26, 16, v20\n\tv_and_b32_e32 v27, 0xffff0000, v20\n\tv_add_f32_e32 v30, v28, v26\n\tv_add_f32_e32 v31, v29, v27\n\tds_write_b32 v40, v30 offset:0\n\tds_write_b32 v40, v31 offset:64\n\tv_lshlrev_b32_e32 v26, 16, v21\n\tv_and_b32_e32 v27, 0xffff0000, v21\n\tv_add_f32_e32 v28, v30, v26\n\tv_add_f32_e32 v29, v31, v27\n\tds_write_b32 v40, v28 offset:2048\n\tds_write_b32 v40, v29 offset:2112\n\tv_lshlrev_b32_e32 v26, 16, v22\n\tv_and_b32_e32 v27, 0xffff0000, v22\n\tv_add_f32_e32 v30, v28, v26\n\tv_add_f32_e32 v31, v29, v27\n\tds_write_b32 v40, v30 offset:4096\n\tds_write_b32 v40, v31 offset:4160\n\tv_lshlrev_b32_e32 v26, 16, v23\n\tv_and_b32_e32 v27, 0xffff0000, v23\n\tv_add_f32_e32 v28, v30, v26\n\tv_add_f32_e32 v29, v31, v27\n\tds_write_b32 v40, v28 offset:6144\n\tds_write_b32 v40, v29 offset:6208\n\tv_lshlrev_b32_e32 v26, 16, v24\n\tv_and_b32_e32 v27, 0xffff0000, v24\n\tv_add_f32_e32 v30, v28, v26\n\tv_add_f32_e32 v31, v29, v27\n\tds_write_b32 v40, v30 offset:8192\n\tds_write_b32 v40, v31 offset:8256\n\tv_lshlrev_b32_e32 v26, 16, v25\n\tv_and_b32_e32 v27, 0xffff0000, v25\n\tv_add_f32_e32 v28, v30, v26\n\tv_add_f32_e32 v29, v31, v27\n\tds_write_b32 v40, v28 offset:10240\n\tds_write_b32 v40, v29 offset:10304\n\t94:\n\t"
    "s_add_u32 s42, %[lds], 81920\n\t"
    "v_mov_b32_e32 v14, s42\n\t"
    "s_waitcnt lgkmcnt(0)\n\t"
    "s_barrier\n\t"
    "ds_read_b128 a[0:3], v112 offset:0\n\t"
    "ds_read_b128 a[4:7], v112 offset:1024\n\t"
    "ds_read_b128 a[8:11], v112 offset:2048\n\t"
    "ds_read_b128 a[12:15], v112 offset:3072\n\t"
    "ds_read_b32 v13, v14 offset:4\n\t"
    "ds_read_b128 a[32:35], v113 offset:4096\n\t"
    "ds_read_b128 a[36:39], v113 offset:5120\n\t"
    "ds_read_b128 a[40:43], v113 offset:6144\n\t"
    "ds_read_b128 a[44:47], v113 offset:7168\n\t"
    "s_waitcnt vmcnt(8)\n\t"
    "v_perm_b32 v240, v196, v192, s53\n\t"
    "v_perm_b32 v244, v196, v192, s54\n\t"
    "v_perm_b32 v241, v204, v200, s53\n\t"
    "v_perm_b32 v245, v204, v200, s54\n\t"
    "v_perm_b32 v242, v212, v208, s53\n\t"
    "v_perm_b32 v246, v212, v208, s54\n\t"
    "v_perm_b32 v243, v220, v216, s53\n\t"
    "v_perm_b32 v247, v220, v216, s54\n\t"
    "s_mov_b32 s41, 4\n\t"
    "s_waitcnt lgkmcnt(8)\n\t"
    "v_mfma_f32_16x16x32_bf16 v[16:19], v[240:243], a[0:3], 0\n\t"
    "v_perm_b32 v224, v197, v193, s53\n\t"
    "v_perm_b32 v228, v197, v193, s54\n\t"
    "v_perm_b32 v225, v205, v201, s53\n\t"
    "v_mfma_f32_16x16x32_bf16 v[32:35], v[244:247], a[0:3], 0\n\t"
    "v_perm_b32 v229, v205, v201, s54\n\t"
    "v_perm_b32 v226, v213, v209, s53\n\t"
    "v_perm_b32 v230, v213, v209, s54\n\t"
    "s_waitcnt lgkmcnt(7)\n\t"
    "v_mfma_f32_16x16x32_bf16 v[36:39], v[244:247], a[4:7], 0\n\t"
    "v_perm_b32 v227, v221, v217, s53\n\t"
    "v_perm_b32 v231, v221, v217, s54\n\t"
    "s_add_u32 s55, 53248, s52\n\t"
    "v_mfma_f32_16x16x32_bf16 v[20:23], v[240:243], a[4:7], 0\n\t"
    "buffer_load_dwordx3 v[160:162], v252, %[sS], s55 offen offset:0\n\t"
    "buffer_load_dwordx3 v[164:166], v252, %[sS], s55 offen offset:832\n\t"
    "buffer_load_dwordx3 v[168:170], v252, %[sS], s55 offen offset:1664\n\t"
    "s_waitcnt lgkmcnt(6)\n\t"
    "v_mfma_f32_16x16x32_bf16 v[24:27], v[240:243], a[8:11], 0\n\t"
    "buffer_load_dwordx3 v[172:174], v252, %[sS], s55 offen offset:2496\n\t"
    "buffer_load_dwordx3 v[176:178], v253, %[sS], s55 offen offset:0\n\t"
    "buffer_load_dwordx3 v[180:182], v253, %[sS], s55 offen offset:832\n\t"
    "s_cmp_lg_u32 %[wv], 3\n\ts_cbranch_scc1 85f\n\ts_waitcnt vmcnt(6)\n\tv_accvgpr_read_b32 v116, a240\n\tv_accvgpr_read_b32 v117, a241\n\tv_accvgpr_read_b32 v118, a242\n\tv_accvgpr_read_b32 v119, a243\n\tv_accvgpr_read_b32 v120, a244\n\tv_accvgpr_read_b32 v121, a245\n\tv_accvgpr_read_b32 v122, a246\n\tv_accvgpr_read_b32 v123, a247\n\tv_perm_b32 v124, v117, v116, s53\n\tv_perm_b32 v125, v119, v118, s53\n\tv_perm_b32 v126, v121, v120, s53\n\tv_perm_b32 v127, v123, v122, s53\n\tbuffer_store_dwordx4 v[124:127], v15, %[sW], %[bD0] offen sc1\n\ts_nop 1\n\tv_perm_b32 v124, v117, v116, s54\n\tv_perm_b32 v125, v119, v118, s54\n\tv_perm_b32 v126, v121, v120, s54\n\tv_perm_b32 v127, v123, v122, s54\n\tbuffer_store_dwordx4 v[124:127], v15, %[sW], %[bD1] offen sc1\n\tv_accvgpr_read_b32 v116, a208\n\tv_accvgpr_read_b32 v117, a212\n\tv_accvgpr_read_b32 v118, a216\n\tv_accvgpr_read_b32 v119, a220\n\tv_accvgpr_read_b32 v120, a224\n\tv_accvgpr_read_b32 v121, a228\n\tv_accvgpr_read_b32 v122, a232\n\tv_accvgpr_read_b32 v123, a236\n\tv_perm_b32 v124, v117, v116, s53\n\tv_perm_b32 v125, v119, v118, s53\n\tv_perm_b32 v126, v121, v120, s53\n\tv_perm_b32 v127, v123, v122, s53\n\tbuffer_store_dwordx4 v[124:127], v15, %[sW], %[aD0] offen offset:0 sc1\n\ts_nop 1\n\tv_perm_b32 v124, v117, v116, s54\n\tv_perm_b32 v125, v119, v118, s54\n\tv_perm_b32 v126, v121, v120, s54\n\tv_perm_b32 v127, v123, v122, s54\n\tbuffer_store_dwordx4 v[124:127], v15, %[sW], %[aD0] offen offset:1024 sc1\n\tv_accvgpr_read_b32 v116, a209\n\tv_accvgpr_read_b32 v117, a213\n\tv_accvgpr_read_b32 v118, a217\n\tv_accvgpr_read_b32 v119, a221\n\tv_accvgpr_read_b32 v120, a225\n\tv_accvgpr_read_b32 v121, a229\n\tv_accvgpr_read_b32 v122, a233\n\tv_accvgpr_read_b32 v123, a237\n\tv_perm_b32 v124, v117, v116, s53\n\tv_perm_b32 v125, v119, v118, s53\n\tv_perm_b32 v126, v121, v120, s53\n\tv_perm_b32 v127, v123, v122, s53\n\tbuffer_store_dwordx4 v[124:127], v15, %[sW], %[aD1] offen offset:0 sc1\n\ts_nop 1\n\tv_perm_b32 v124, v117, v116, s54\n\tv_perm_b32 v125, v119, v118, s54\n\tv_perm_b32 v126, v121, v120, s54\n\tv_perm_b32 v127, v123, v122, s54\n\tbuffer_store_dwordx4 v[124:127], v15, %[sW], %[aD1] offen offset:1024 sc1\n\tv_accvgpr_read_b32 v116, a210\n\tv_accvgpr_read_b32 v117, a214\n\tv_accvgpr_read_b32 v118, a218\n\tv_accvgpr_read_b32 v119, a222\n\tv_accvgpr_read_b32 v120, a226\n\tv_accvgpr_read_b32 v121, a230\n\tv_accvgpr_read_b32 v122, a234\n\tv_accvgpr_read_b32 v123, a238\n\tv_perm_b32 v124, v117, v116, s53\n\tv_perm_b32 v125, v119, v118, s53\n\tv_perm_b32 v126, v121, v120, s53\n\tv_perm_b32 v127, v123, v122, s53\n\tbuffer_store_dwordx4 v[124:127], v15, %[sW], %[aD2] offen offset:0 sc1\n\ts_nop 1\n\tv_perm_b32 v124, v117, v116, s54\n\tv_perm_b32 v125, v119, v118, s54\n\tv_perm_b32 v126, v121, v120, s54\n\tv_perm_b32 v127, v123, v122, s54\n\tbuffer_store_dwordx4 v[124:127], v15, %[sW], %[aD2] offen offset:1024 sc1\n\ts_cmp_eq_u32 %[bZ], 0\n\ts_cbranch_scc1 85f\n\tv_mov_b32_e32 v116, 0\n\tv_mov_b32_e32 v117, 0\n\tv_mov_b32_e32 v118, 0\n\tv_mov_b32_e32 v119, 0\n\ts_add_u32 s42, %[bD0], 522240\n\ts_add_u32 s45, %[bD1], 522240\n\tbuffer_store_dwordx4 v[116:119], v15, %[sW], s42 offen sc1\n\tbuffer_store_dwordx4 v[116:119], v15, %[sW], s45 offen sc1\n\t85:\n\t"
    "v_mfma_f32_16x16x32_bf16 v[40:43], v[244:247], a[8:11], 0\n\t"
    "buffer_load_dwordx3 v[184:186], v253, %[sS], s55 offen offset:1664\n\t"
    "buffer_load_dwordx3 v[188:190], v253, %[sS], s55 offen offset:2496\n\t"
    "ds_read_b128 a[16:19], v112 offset:8192\n\t"
    "s_waitcnt lgkmcnt(6)\n\t"
    "v_mfma_f32_16x16x32_bf16 v[44:47], v[244:247], a[12:15], 0\n\t"
    "ds_read_b128 a[20:23], v112 offset:9216\n\t"
    "ds_read_b128 a[24:27], v112 offset:10240\n\t"
    "ds_read_b128 a[28:31], v112 offset:11264\n\t"
    "v_mfma_f32_16x16x32_bf16 v[28:31], v[240:243], a[12:15], 0\n\t"
    "ds_read_b128 a[80:83], v113 offset:12288\n\t"
    "ds_read_b128 a[84:87], v113 offset:13312\n\t"
    "ds_read_b128 a[88:91], v113 offset:14336\n\t"
    "v_mfma_f32_16x16x32_bf16 v[48:51], v[224:227], a[0:3], 0\n\t"
    "v_perm_b32 v232, v198, v194, s53\n\t"
    "v_perm_b32 v236, v198, v194, s54\n\t"
    "ds_read_b128 a[92:95], v113 offset:15360\n\t"
    "v_mfma_f32_16x16x32_bf16 v[64:67], v[228:231], a[0:3], 0\n\t"
    "v_perm_b32 v233, v206, v202, s53\n\t"
    "v_perm_b32 v237, v206, v202, s54\n\t"
    "ds_read_b128 a[48:51], v112 offset:16384\n\t"
    "v_mfma_f32_16x16x32_bf16 v[68:71], v[228:231], a[4:7], 0\n\t"
    "v_perm_b32 v234, v214, v210, s53\n\t"
    "v_perm_b32 v238, v214, v210, s54\n\t"
    "ds_read_b128 a[52:55], v112 offset:17408\n\t"
    "v_mfma_f32_16x16x32_bf16 v[52:55], v[224:227], a[4:7], 0\n\t"
    "v_perm_b32 v235, v222, v218, s53\n\t"
    "v_perm_b32 v239, v222, v218, s54\n\t"
    "ds_read_b128 a[56:59], v112 offset:18432\n\t"
    "v_mfma_f32_16x16x32_bf16 v[56:59], v[224:227], a[8:11], 0\n\t"
    "ds_read_b128 a[60:63], v112 offset:19456\n\t"
    "ds_read_b128 a[128:131], v113 offset:20480\n\t"
    "ds_read_b128 a[132:135], v113 offset:21504\n\t"
    "v_mfma_f32_16x16x32_bf16 v[72:75], v[228:231], a[8:11], 0\n\t"
    "ds_read_b128 a[136:139], v113 offset:22528\n\t"
    "ds_read_b128 a[140:143], v113 offset:23552\n\t"
    "ds_read_b128 a[64:67], v112 offset:24576\n\t"
    "v_mfma_f32_16x16x32_bf16 v[76:79], v[228:231], a[12:15], 0\n\t"
    "ds_read_b128 a[68:71], v112 offset:25600\n\t"
    "ds_read_b128 a[72:75], v112 offset:26624\n\t"
    "ds_read_b128 a[76:79], v112 offset:27648\n\t"
    "v_mfma_f32_16x16x32_bf16 v[60:63], v[224:227], a[12:15], 0\n\t"
    "ds_read_b128 a[96:99], v113 offset:28672\n\t"
    "ds_read_b128 a[100:103], v113 offset:29696\n\t"
    "ds_read_b128 a[104:107], v113 offset:30720\n\t"
    "v_mfma_f32_16x16x32_bf16 v[80:83], v[232:235], a[0:3], 0\n\t"
    "s_waitcnt vmcnt(8)\n\t"
    "v_perm_b32 v240, v132, v128, s53\n\t"
    "v_perm_b32 v244, v132, v128, s54\n\t"
    "v_perm_b32 v241, v140, v136, s53\n\t"
    "v_mfma_f32_16x16x32_bf16 v[96:99], v[236:239], a[0:3], 0\n\t"
    "v_perm_b32 v245, v140, v136, s54\n\t"
    "v_perm_b32 v242, v148, v144, s53\n\t"
    "v_perm_b32 v246, v148, v144, s54\n\t"
    "v_mfma_f32_16x16x32_bf16 v[100:103], v[236:239], a[4:7], 0\n\t"
    "v_perm_b32 v243, v156, v152, s53\n\t"
    "v_perm_b32 v247, v156, v152, s54\n\t"
    "s_sub_u32 s55, 79872, s52\n\t"
    "v_mfma_f32_16x16x32_bf16 v[84:87], v[232:235], a[4:7], 0\n\t"
    "buffer_load_dwordx3 v[192:194], v252, %[sS], s55 offen offset:0\n\t"
    "buffer_load_dwordx3 v[196:198], v252, %[sS], s55 offen offset:832\n\t"
    "buffer_load_dwordx3 v[200:202], v252, %[sS], s55 offen offset:1664\n\t"
    "v_mfma_f32_16x16x32_bf16 v[88:91], v[232:235], a[8:11], 0\n\t"
    "buffer_load_dwordx3 v[204:206], v252, %[sS], s55 offen offset:2496\n\t"
    "buffer_load_dwordx3 v[208:210], v253, %[sS], s55 offen offset:0\n\t"
    "buffer_load_dwordx3 v[212:214], v253, %[sS], s55 offen offset:832\n\t"
    "v_mfma_f32_16x16x32_bf16 v[104:107], v[236:239], a[8:11], 0\n\t"
    "buffer_load_dwordx3 v[216:218], v253, %[sS], s55 offen offset:1664\n\t"
    "buffer_load_dwordx3 v[220:222], v253, %[sS], s55 offen offset:2496\n\t"
    "ds_read_b128 a[108:111], v113 offset:31744\n\t"
    "v_mfma_f32_16x16x32_bf16 v[108:111], v[236:239], a[12:15], 0\n\t"
    "ds_read_b128 a[112:115], v112 offset:32768\n\t"
    "ds_read_b128 a[116:119], v112 offset:33792\n\t"
    "ds_read_b128 a[120:123], v112 offset:34816\n\t"
    "v_mfma_f32_16x16x32_bf16 v[92:95], v[232:235], a[12:15], 0\n\t"
    "ds_read_b128 a[124:127], v112 offset:35840\n\t"
    "ds_read_b128 a[144:147], v113 offset:36864\n\t"
    "ds_read_b128 a[148:151], v113 offset:37888\n\t"
    "s_waitcnt lgkmcnt(15)\n\t"
    "v_mfma_f32_16x16x32_bf16 v[16:19], v[240:243], a[32:35], v[16:19]\n\t"
    "v_perm_b32 v224, v133, v129, s53\n\t"
    "v_perm_b32 v228, v133, v129, s54\n\t"
    "ds_read_b128 a[152:155], v113 offset:38912\n\t"
    "v_mfma_f32_16x16x32_bf16 v[32:35], v[244:247], a[32:35], v[32:35]\n\t"
    "v_perm_b32 v225, v141, v137, s53\n\t"
    "v_perm_b32 v229, v141, v137, s54\n\t"
    "ds_read_b128 a[156:159], v113 offset:39936\n\t"
    "v_mfma_f32_16x16x32_bf16 v[36:39], v[244:247], a[36:39], v[36:39]\n\t"
    "v_perm_b32 v226, v149, v145, s53\n\t"
    "v_perm_b32 v230, v149, v145, s54\n\t"
    "ds_read_b128 a[160:163], v112 offset:40960\n\t"
    "v_mfma_f32_16x16x32_bf16 v[20:23], v[240:243], a[36:39], v[20:23]\n\t"
    "v_perm_b32 v227, v157, v153, s53\n\t"
    "v_perm_b32 v231, v157, v153, s54\n\t"
    "ds_read_b128 a[164:167], v112 offset:41984\n\t"
    "v_mfma_f32_16x16x32_bf16 v[24:27], v[240:243], a[40:43], v[24:27]\n\t"
    "ds_read_b128 a[168:171], v112 offset:43008\n\t"
    "ds_read_b128 a[172:175], v112 offset:44032\n\t"
    "ds_read_b128 a[176:179], v113 offset:45056\n\t"
    "v_mfma_f32_16x16x32_bf16 v[40:43], v[244:247], a[40:43], v[40:43]\n\t"
    "ds_read_b128 a[180:183], v113 offset:46080\n\t"
    "ds_read_b128 a[184:187], v113 offset:47104\n\t"
    "ds_read_b128 a[188:191], v113 offset:48128\n\t"
    "v_mfma_f32_16x16x32_bf16 v[44:47], v[244:247], a[44:47], v[44:47]\n\t"
    "ds_read_b128 a[192:195], v114 offset:49152\n\t"
    "ds_read_b128 a[196:199], v114 offset:50176\n\t"
    "ds_read_b128 a[200:203], v114 offset:51200\n\t"
    "v_mfma_f32_16x16x32_bf16 v[28:31], v[240:243], a[44:47], v[28:31]\n\t"
    "ds_read_b128 a[204:207], v114 offset:52224\n\t"
    "v_readfirstlane_b32 s51, v13\n\ts_nop 0\n\ts_mov_b32 %[tag], s51\n\t"
    "v_add_u32_e32 v112, 82176, v9\n\t"
    "v_mfma_f32_16x16x32_bf16 v[48:51], v[224:227], a[32:35], v[48:51]\n\t"
    "v_perm_b32 v232, v134, v130, s53\n\t"
    "v_perm_b32 v236, v134, v130, s54\n\t"
    "ds_read_b128 a[248:251], v112 offset:0\n\t"
    "v_mfma_f32_16x16x32_bf16 v[64:67], v[228:231], a[32:35], v[64:67]\n\t"
    "v_perm_b32 v233, v142, v138, s53\n\t"
    "v_perm_b32 v237, v142, v138, s54\n\t"
    "v_mfma_f32_16x16x32_bf16 v[68:71], v[228:231], a[36:39], v[68:71]\n\t"
    "v_perm_b32 v234, v150, v146, s53\n\t"
    "v_perm_b32 v238, v150, v146, s54\n\t"
    "v_mfma_f32_16x16x32_bf16 v[52:55], v[224:227], a[36:39], v[52:55]\n\t"
    "v_perm_b32 v235, v158, v154, s53\n\t"
    "v_perm_b32 v239, v158, v154, s54\n\t"
    "v_mfma_f32_16x16x32_bf16 v[56:59], v[224:227], a[40:43], v[56:59]\n\t"
    "v_mfma_f32_16x16x32_bf16 v[72:75], v[228:231], a[40:43], v[72:75]\n\t"
    "v_mfma_f32_16x16x32_bf16 v[76:79], v[228:231], a[44:47], v[76:79]\n\t"
    "v_mfma_f32_16x16x32_bf16 v[60:63], v[224:227], a[44:47], v[60:63]\n\t"
    "v_mfma_f32_16x16x32_bf16 v[80:83], v[232:235], a[32:35], v[80:83]\n\t"
    "s_waitcnt vmcnt(8)\n\t"
    "v_perm_b32 v240, v164, v160, s53\n\t"
    "v_perm_b32 v244, v164, v160, s54\n\t"
    "v_perm_b32 v241, v172, v168, s53\n\t"
    "v_mfma_f32_16x16x32_bf16 v[96:99], v[236:239], a[32:35], v[96:99]\n\t"
    "v_perm_b32 v245, v172, v168, s54\n\t"
    "v_perm_b32 v242, v180, v176, s53\n\t"
    "v_perm_b32 v246, v180, v176, s54\n\t"
    "v_mfma_f32_16x16x32_bf16 v[100:103], v[236:239], a[36:39], v[100:103]\n\t"
    "v_perm_b32 v243, v188, v184, s53\n\t"
    "v_perm_b32 v247, v188, v184, s54\n\t"
    "s_add_u32 s55, 106496, s52\n\t"
    "v_mfma_f32_16x16x32_bf16 v[84:87], v[232:235], a[36:39], v[84:87]\n\t"
    "buffer_load_dwordx3 v[128:130], v252, %[sS], s55 offen offset:0\n\t"
    "buffer_load_dwordx3 v[132:134], v252, %[sS], s55 offen offset:832\n\t"
    "buffer_load_dwordx3 v[136:138], v252, %[sS], s55 offen offset:1664\n\t"
    "v_mfma_f32_16x16x32_bf16 v[88:91], v[232:235], a[40:43], v[88:91]\n\t"
    "buffer_load_dwordx3 v[140:142], v252, %[sS], s55 offen offset:2496\n\t"
    "buffer_load_dwordx3 v[144:146], v253, %[sS], s55 offen offset:0\n\t"
    "buffer_load_dwordx3 v[148:150], v253, %[sS], s55 offen offset:832\n\t"
    "v_mfma_f32_16x16x32_bf16 v[104:107], v[236:239], a[40:43], v[104:107]\n\t"
    "buffer_load_dwordx3 v[152:154], v253, %[sS], s55 offen offset:1664\n\t"
    "buffer_load_dwordx3 v[156:158], v253, %[sS], s55 offen offset:2496\n\t"
    "v_mfma_f32_16x16x32_bf16 v[108:111], v[236:239], a[44:47], v[108:111]\n\t"
    "v_mfma_f32_16x16x32_bf16 v[92:95], v[232:235], a[44:47], v[92:95]\n\t"
    "v_mfma_f32_16x16x32_bf16 v[16:19], v[240:243], a[16:19], v[16:19]\n\t"
    "v_perm_b32 v224, v165, v161, s53\n\t"
    "v_perm_b32 v228, v165, v161, s54\n\t"
    "v_mfma_f32_16x16x32_bf16 v[32:35], v[244:247], a[16:19], v[32:35]\n\t"
    "v_perm_b32 v225, v173, v169, s53\n\t"
    "v_perm_b32 v229, v173, v169, s54\n\t"
    "v_mfma_f32_16x16x32_bf16 v[36:39], v[244:247], a[20:23], v[36:39]\n\t"
    "v_perm_b32 v226, v181, v177, s53\n\t"
    "v_perm_b32 v230, v181, v177, s54\n\t"
    "v_mfma_f32_16x16x32_bf16 v[20:23], v[240:243], a[20:23], v[20:23]\n\t"
    "v_perm_b32 v227, v189, v185, s53\n\t"
    "v_perm_b32 v231, v189, v185, s54\n\t"
    "v_mfma_f32_16x16x32_bf16 v[24:27], v[240:243], a[24:27], v[24:27]\n\t"
    "v_mfma_f32_16x16x32_bf16 v[40:43], v[244:247], a[24:27], v[40:43]\n\t"
    "v_mfma_f32_16x16x32_bf16 v[44:47], v[244:247], a[28:31], v[44:47]\n\t"
    "v_mfma_f32_16x16x32_bf16 v[28:31], v[240:243], a[28:31], v[28:31]\n\t"
    "v_mfma_f32_16x16x32_bf16 v[48:51], v[224:227], a[16:19], v[48:51]\n\t"
    "v_perm_b32 v232, v166, v162, s53\n\t"
    "v_perm_b32 v236, v166, v162, s54\n\t"
    "v_mfma_f32_16x16x32_bf16 v[64:67], v[228:231], a[16:19], v[64:67]\n\t"
    "v_perm_b32 v233, v174, v170, s53\n\t"
    "v_perm_b32 v237, v174, v170, s54\n\t"
    "v_mfma_f32_16x16x32_bf16 v[68:71], v[228:231], a[20:23], v[68:71]\n\t"
    "v_perm_b32 v234, v182, v178, s53\n\t"
    "v_perm_b32 v238, v182, v178, s54\n\t"
    "v_mfma_f32_16x16x32_bf16 v[52:55], v[224:227], a[20:23], v[52:55]\n\t"
    "v_perm_b32 v235, v190, v186, s53\n\t"
    "v_perm_b32 v239, v190, v186, s54\n\t"
    "v_mfma_f32_16x16x32_bf16 v[56:59], v[224:227], a[24:27], v[56:59]\n\t"
    "v_mfma_f32_16x16x32_bf16 v[72:75], v[228:231], a[24:27], v[72:75]\n\t"
    "v_mfma_f32_16x16x32_bf16 v[76:79], v[228:231], a[28:31], v[76:79]\n\t"
    "v_mfma_f32_16x16x32_bf16 v[60:63], v[224:227], a[28:31], v[60:63]\n\t"
    "v_mfma_f32_16x16x32_bf16 v[80:83], v[232:235], a[16:19], v[80:83]\n\t"
    "s_waitcnt vmcnt(8)\n\t"
    "v_perm_b32 v240, v196, v192, s53\n\t"
    "v_perm_b32 v244, v196, v192, s54\n\t"
    "v_perm_b32 v241, v204, v200, s53\n\t"
    "v_mfma_f32_16x16x32_bf16 v[96:99], v[236:239], a[16:19], v[96:99]\n\t"
    "v_perm_b32 v245, v204, v200, s54\n\t"
    "v_perm_b32 v242, v212, v208, s53\n\t"
    "v_perm_b32 v246, v212, v208, s54\n\t"
    "v_mfma_f32_16x16x32_bf16 v[100:103], v[236:239], a[20:23], v[100:103]\n\t"
    "v_perm_b32 v243, v220, v216, s53\n\t"
    "v_perm_b32 v247, v220, v216, s54\n\t"
    "s_sub_u32 s55, 133120, s52\n\t"
    "v_mfma_f32_16x16x32_bf16 v[84:87], v[232:235], a[20:23], v[84:87]\n\t"
    "buffer_load_dwordx3 v[160:162], v252, %[sS], s55 offen offset:0\n\t"
    "buffer_load_dwordx3 v[164:166], v252, %[sS], s55 offen offset:832\n\t"
    "buffer_load_dwordx3 v[168:170], v252, %[sS], s55 offen offset:1664\n\t"
    "v_mfma_f32_16x16x32_bf16 v[88:91], v[232:235], a[24:27], v[88:91]\n\t"
    "buffer_load_dwordx3 v[172:174], v252, %[sS], s55 offen offset:2496\n\t"
    "buffer_load_dwordx3 v[176:178], v253, %[sS], s55 offen offset:0\n\t"
    "buffer_load_dwordx3 v[180:182], v253, %[sS], s55 offen offset:832\n\t"
    "v_mfma_f32_16x16x32_bf16 v[104:107], v[236:239], a[24:27], v[104:107]\n\t"
    "buffer_load_dwordx3 v[184:186], v253, %[sS], s55 offen offset:1664\n\t"
    "buffer_load_dwordx3 v[188:190], v253, %[sS], s55 offen offset:2496\n\t"
    "v_mfma_f32_16x16x32_bf16 v[108:111], v[236:239], a[28:31], v[108:111]\n\t"
    "v_mfma_f32_16x16x32_bf16 v[92:95], v[232:235], a[28:31], v[92:95]\n\t"
    "v_mfma_f32_16x16x32_bf16 v[16:19], v[240:243], a[80:83], v[16:19]\n\t"
    "v_perm_b32 v224, v197, v193, s53\n\t"
    "v_perm_b32 v228, v197, v193, s54\n\t"
    "v_mfma_f32_16x16x32_bf16 v[32:35], v[244:247], a[80:83], v[32:35]\n\t"
    "v_perm_b32 v225, v205, v201, s53\n\t"
    "v_perm_b32 v229, v205, v201, s54\n\t"
    "v_mfma_f32_16x16x32_bf16 v[36:39], v[244:247], a[84:87], v[36:39]\n\t"
    "v_perm_b32 v226, v213, v209, s53\n\t"
    "v_perm_b32 v230, v213, v209, s54\n\t"
    "v_mfma_f32_16x16x32_bf16 v[20:23], v[240:243], a[84:87], v[20:23]\n\t"
    "v_perm_b32 v227, v221, v217, s53\n\t"
    "v_perm_b32 v231, v221, v217, s54\n\t"
    "v_mfma_f32_16x16x32_bf16 v[24:27], v[240:243], a[88:91], v[24:27]\n\t"
    "v_mfma_f32_16x16x32_bf16 v[40:43], v[244:247], a[88:91], v[40:43]\n\t"
    "v_mfma_f32_16x16x32_bf16 v[44:47], v[244:247], a[92:95], v[44:47]\n\t"
    "v_mfma_f32_16x16x32_bf16 v[28:31], v[240:243], a[92:95], v[28:31]\n\t"
    "v_mfma_f32_16x16x32_bf16 v[48:51], v[224:227], a[80:83], v[48:51]\n\t"
    "v_perm_b32 v232, v198, v194, s53\n\t"
    "v_perm_b32 v236, v198, v194, s54\n\t"
    "v_mfma_f32_16x16x32_bf16 v[64:67], v[228:231], a[80:83], v[64:67]\n\t"
    "v_perm_b32 v233, v206, v202, s53\n\t"
    "v_perm_b32 v237, v206, v202, s54\n\t"
    "v_mfma_f32_16x16x32_bf16 v[68:71], v[228:231], a[84:87], v[68:71]\n\t"
    "v_perm_b32 v234, v214, v210, s53\n\t"
    "v_perm_b32 v238, v214, v210, s54\n\t"
    "v_mfma_f32_16x16x32_bf16 v[52:55], v[224:227], a[84:87], v[52:55]\n\t"
    "v_perm_b32 v235, v222, v218, s53\n\t"
    "v_perm_b32 v239, v222, v218, s54\n\t"
    "v_mfma_f32_16x16x32_bf16 v[56:59], v[224:227], a[88:91], v[56:59]\n\t"
    "v_mfma_f32_16x16x32_bf16 v[72:75], v[228:231], a[88:91], v[72:75]\n\t"
    "v_mfma_f32_16x16x32_bf16 v[76:79], v[228:231], a[92:95], v[76:79]\n\t"
    "v_mfma_f32_16x16x32_bf16 v[60:63], v[224:227], a[92:95], v[60:63]\n\t"
    "v_mfma_f32_16x16x32_bf16 v[80:83], v[232:235], a[80:83], v[80:83]\n\t"
    "s_waitcnt vmcnt(8)\n\t"
    "v_perm_b32 v240, v132, v128, s53\n\t"
    "v_perm_b32 v244, v132, v128, s54\n\t"
    "v_perm_b32 v241, v140, v136, s53\n\t"
    "s_cmp_lg_u32 %[wv], 3\n\ts_cbranch_scc1 86f\n\tv_mov_b32_e32 v14, s51\n\ts_lshr_b32 s42, %[blk], 3\n\ts_lshl_b32 s42, s42, 7\n\ts_and_b32 s45, %[blk], 7\n\ts_lshl_b32 s45, s45, 2\n\ts_add_u32 s42, s42, s45\n\ts_add_u32 s42, s42, 2121856\n\tv_mov_b32_e32 v15, s42\n\ts_mov_b64 exec, 1\n\tbuffer_store_dword v14, v15, %[sW], 0 offen sc1\n\ts_mov_b64 exec, -1\n\t86:\n\t"
    "v_mfma_f32_16x16x32_bf16 v[96:99], v[236:239], a[80:83], v[96:99]\n\t"
    "v_perm_b32 v245, v140, v136, s54\n\t"
    "v_perm_b32 v242, v148, v144, s53\n\t"
    "v_perm_b32 v246, v148, v144, s54\n\t"
    "v_mfma_f32_16x16x32_bf16 v[100:103], v[236:239], a[84:87], v[100:103]\n\t"
    "v_perm_b32 v243, v156, v152, s53\n\t"
    "v_perm_b32 v247, v156, v152, s54\n\t"
    "s_add_u32 s55, 159744, s52\n\t"
    "v_mfma_f32_16x16x32_bf16 v[84:87], v[232:235], a[84:87], v[84:87]\n\t"
    "buffer_load_dwordx3 v[192:194], v252, %[sS], s55 offen offset:0\n\t"
    "buffer_load_dwordx3 v[196:198], v252, %[sS], s55 offen offset:832\n\t"
    "buffer_load_dwordx3 v[200:202], v252, %[sS], s55 offen offset:1664\n\t"
    "v_mfma_f32_16x16x32_bf16 v[88:91], v[232:235], a[88:91], v[88:91]\n\t"
    "buffer_load_dwordx3 v[204:206], v252, %[sS], s55 offen offset:2496\n\t"
    "buffer_load_dwordx3 v[208:210], v253, %[sS], s55 offen offset:0\n\t"
    "buffer_load_dwordx3 v[212:214], v253, %[sS], s55 offen offset:832\n\t"
    "v_mfma_f32_16x16x32_bf16 v[104:107], v[236:239], a[88:91], v[104:107]\n\t"
    "buffer_load_dwordx3 v[216:218], v253, %[sS], s55 offen offset:1664\n\t"
    "buffer_load_dwordx3 v[220:222], v253, %[sS], s55 offen offset:2496\n\t"
    "v_mfma_f32_16x16x32_bf16 v[108:111], v[236:239], a[92:95], v[108:111]\n\t"
    "v_mfma_f32_16x16x32_bf16 v[92:95], v[232:235], a[92:95], v[92:95]\n\t"
    "v_mfma_f32_16x16x32_bf16 v[16:19], v[240:243], a[48:51], v[16:19]\n\t"
    "v_perm_b32 v224, v133, v129, s53\n\t"
    "v_perm_b32 v228, v133, v129, s54\n\t"
    "v_mfma_f32_16x16x32_bf16 v[32:35], v[244:247], a[48:51], v[32:35]\n\t"
    "v_perm_b32 v225, v141, v137, s53\n\t"
    "v_perm_b32 v229, v141, v137, s54\n\t"
    "v_mfma_f32_16x16x32_bf16 v[36:39], v[244:247], a[52:55], v[36:39]\n\t"
    "v_perm_b32 v226, v149, v145, s53\n\t"
    "v_perm_b32 v230, v149, v145, s54\n\t"
    "v_mfma_f32_16x16x32_bf16 v[20:23], v[240:243], a[52:55], v[20:23]\n\t"
    "v_perm_b32 v227, v157, v153, s53\n\t"
    "v_perm_b32 v231, v157, v153, s54\n\t"
    "v_mfma_f32_16x16x32_bf16 v[24:27], v[240:243], a[56:59], v[24:27]\n\t"
    "v_mfma_f32_16x16x32_bf16 v[40:43], v[244:247], a[56:59], v[40:43]\n\t"
    "v_mfma_f32_16x16x32_bf16 v[44:47], v[244:247], a[60:63], v[44:47]\n\t"
    "v_mfma_f32_16x16x32_bf16 v[28:31], v[240:243], a[60:63], v[28:31]\n\t"
    "v_mfma_f32_16x16x32_bf16 v[48:51], v[224:227], a[48:51], v[48:51]\n\t"
    "v_perm_b32 v232, v134, v130, s53\n\t"
    "v_perm_b32 v236, v134, v130, s54\n\t"
    "v_mfma_f32_16x16x32_bf16 v[64:67], v[228:231], a[48:51], v[64:67]\n\t"
    "v_perm_b32 v233, v142, v138, s53\n\t"
    "v_perm_b32 v237, v142, v138, s54\n\t"
    "v_mfma_f32_16x16x32_bf16 v[68:71], v[228:231], a[52:55], v[68:71]\n\t"
    "v_perm_b32 v234, v150, v146, s53\n\t"
    "v_perm_b32 v238, v150, v146, s54\n\t"
    "v_mfma_f32_16x16x32_bf16 v[52:55], v[224:227], a[52:55], v[52:55]\n\t"
    "v_perm_b32 v235, v158, v154, s53\n\t"
    "v_perm_b32 v239, v158, v154, s54\n\t"
    "v_mfma_f32_16x16x32_bf16 v[56:59], v[224:227], a[56:59], v[56:59]\n\t"
    "v_mfma_f32_16x16x32_bf16 v[72:75], v[228:231], a[56:59], v[72:75]\n\t"
    "v_mfma_f32_16x16x32_bf16 v[76:79], v[228:231], a[60:63], v[76:79]\n\t"
    "v_mfma_f32_16x16x32_bf16 v[60:63], v[224:227], a[60:63], v[60:63]\n\t"
    "v_mfma_f32_16x16x32_bf16 v[80:83], v[232:235], a[48:51], v[80:83]\n\t"
    "s_waitcnt vmcnt(8)\n\t"
    "v_perm_b32 v240, v164, v160, s53\n\t"
    "v_perm_b32 v244, v164, v160, s54\n\t"
    "v_perm_b32 v241, v172, v168, s53\n\t"
    "v_mfma_f32_16x16x32_bf16 v[96:99], v[236:239], a[48:51], v[96:99]\n\t"
    "v_perm_b32 v245, v172, v168, s54\n\t"
    "v_perm_b32 v242, v180, v176, s53\n\t"
    "v_perm_b32 v246, v180, v176, s54\n\t"
    "v_mfma_f32_16x16x32_bf16 v[100:103], v[236:239], a[52:55], v[100:103]\n\t"
    "v_perm_b32 v243, v188, v184, s53\n\t"
    "v_perm_b32 v247, v188, v184, s54\n\t"
    "s_sub_u32 s55, 186368, s52\n\t"
    "v_mfma_f32_16x16x32_bf16 v[84:87], v[232:235], a[52:55], v[84:87]\n\t"
    "buffer_load_dwordx3 v[128:130], v252, %[sS], s55 offen offset:0\n\t"
    "buffer_load_dwordx3 v[132:134], v252, %[sS], s55 offen offset:832\n\t"
    "buffer_load_dwordx3 v[136:138], v252, %[sS], s55 offen offset:1664\n\t"
    "v_mfma_f32_16x16x32_bf16 v[88:91], v[232:235], a[56:59], v[88:91]\n\t"
    "buffer_load_dwordx3 v[140:142], v252, %[sS], s55 offen offset:2496\n\t"
    "buffer_load_dwordx3 v[144:146], v253, %[sS], s55 offen offset:0\n\t"
    "buffer_load_dwordx3 v[148:150], v253, %[sS], s55 offen offset:832\n\t"
    "v_mfma_f32_16x16x32_bf16 v[104:107], v[236:239], a[56:59], v[104:107]\n\t"
    "buffer_load_dwordx3 v[152:154], v253, %[sS], s55 offen offset:1664\n\t"
    "buffer_load_dwordx3 v[156:158], v253, %[sS], s55 offen offset:2496\n\t"
    "v_mfma_f32_16x16x32_bf16 v[108:111], v[236:239], a[60:63], v[108:111]\n\t"
    "v_mfma_f32_16x16x32_bf16 v[92:95], v[232:235], a[60:63], v[92:95]\n\t"
    "v_mfma_f32_16x16x32_bf16 v[16:19], v[240:243], a[128:131], v[16:19]\n\t"
    "v_perm_b32 v224, v165, v161, s53\n\t"
    "v_perm_b32 v228, v165, v161, s54\n\t"
    "v_mfma_f32_16x16x32_bf16 v[32:35], v[244:247], a[128:131], v[32:35]\n\t"
    "v_perm_b32 v225, v173, v169, s53\n\t"
    "v_perm_b32 v229, v173, v169, s54\n\t"
    "v_mfma_f32_16x16x32_bf16 v[36:39], v[244:247], a[132:135], v[36:39]\n\t"
    "v_perm_b32 v226, v181, v177, s53\n\t"
    "v_perm_b32 v230, v181, v177, s54\n\t"
    "v_mfma_f32_16x16x32_bf16 v[20:23], v[240:243], a[132:135], v[20:23]\n\t"
    "v_perm_b32 v227, v189, v185, s53\n\t"
    "v_perm_b32 v231, v189, v185, s54\n\t"
    "v_mfma_f32_16x16x32_bf16 v[24:27], v[240:243], a[136:139], v[24:27]\n\t"
    "v_mfma_f32_16x16x32_bf16 v[40:43], v[244:247], a[136:139], v[40:43]\n\t"
    "s_waitcnt lgkmcnt(15)\n\t"
    "v_mfma_f32_16x16x32_bf16 v[44:47], v[244:247], a[140:143], v[44:47]\n\t"
    "v_mfma_f32_16x16x32_bf16 v[28:31], v[240:243], a[140:143], v[28:31]\n\t"
    "v_mfma_f32_16x16x32_bf16 v[48:51], v[224:227], a[128:131], v[48:51]\n\t"
    "v_perm_b32 v232, v166, v162, s53\n\t"
    "v_perm_b32 v236, v166, v162, s54\n\t"
    "v_mfma_f32_16x16x32_bf16 v[64:67], v[228:231], a[128:131], v[64:67]\n\t"
    "v_perm_b32 v233, v174, v170, s53\n\t"
    "v_perm_b32 v237, v174, v170, s54\n\t"
    "v_mfma_f32_16x16x32_bf16 v[68:71], v[228:231], a[132:135], v[68:71]\n\t"
    "v_perm_b32 v234, v182, v178, s53\n\t"
    "v_perm_b32 v238, v182, v178, s54\n\t"
    "v_mfma_f32_16x16x32_bf16 v[52:55], v[224:227], a[132:135], v[52:55]\n\t"
    "v_perm_b32 v235, v190, v186, s53\n\t"
    "v_perm_b32 v239, v190, v186, s54\n\t"
    "v_mfma_f32_16x16x32_bf16 v[56:59], v[224:227], a[136:139], v[56:59]\n\t"
    "v_mfma_f32_16x16x32_bf16 v[72:75], v[228:231], a[136:139], v[72:75]\n\t"
    "v_mfma_f32_16x16x32_bf16 v[76:79], v[228:231], a[140:143], v[76:79]\n\t"
    "v_mfma_f32_16x16x32_bf16 v[60:63], v[224:227], a[140:143], v[60:63]\n\t"
    "v_mfma_f32_16x16x32_bf16 v[80:83], v[232:235], a[128:131], v[80:83]\n\t"
    "s_waitcnt vmcnt(8)\n\t"
    "v_perm_b32 v240, v196, v192, s53\n\t"
    "v_perm_b32 v244, v196, v192, s54\n\t"
    "v_perm_b32 v241, v204, v200, s53\n\t"
    "v_mfma_f32_16x16x32_bf16 v[96:99], v[236:239], a[128:131], v[96:99]\n\t"
    "v_perm_b32 v245, v204, v200, s54\n\t"
    "v_perm_b32 v242, v212, v208, s53\n\t"
    "v_perm_b32 v246, v212, v208, s54\n\t"
    "v_mfma_f32_16x16x32_bf16 v[100:103], v[236:239], a[132:135], v[100:103]\n\t"
    "v_perm_b32 v243, v220, v216, s53\n\t"
    "v_perm_b32 v247, v220, v216, s54\n\t"
    "s_add_u32 s55, 212992, s52\n\t"
    "v_mfma_f32_16x16x32_bf16 v[84:87], v[232:235], a[132:135], v[84:87]\n\t"
    "buffer_load_dwordx3 v[160:162], v252, %[sS], s55 offen offset:0\n\t"
    "buffer_load_dwordx3 v[164:166], v252, %[sS], s55 offen offset:832\n\t"
    "buffer_load_dwordx3 v[168:170], v252, %[sS], s55 offen offset:1664\n\t"
    "v_mfma_f32_16x16x32_bf16 v[88:91], v[232:235], a[136:139], v[88:91]\n\t"
    "buffer_load_dwordx3 v[172:174], v252, %[sS], s55 offen offset:2496\n\t"
    "buffer_load_dwordx3 v[176:178], v253, %[sS], s55 offen offset:0\n\t"
    "buffer_load_dwordx3 v[180:182], v253, %[sS], s55 offen offset:832\n\t"
    "v_mfma_f32_16x16x32_bf16 v[104:107], v[236:239], a[136:139], v[104:107]\n\t"
    "buffer_load_dwordx3 v[184:186], v253, %[sS], s55 offen offset:1664\n\t"
    "buffer_load_dwordx3 v[188:190], v253, %[sS], s55 offen offset:2496\n\t"
    "v_mfma_f32_16x16x32_bf16 v[108:111], v[236:239], a[140:143], v[108:111]\n\t"
    "v_mfma_f32_16x16x32_bf16 v[92:95], v[232:235], a[140:143], v[92:95]\n\t"
    "v_mfma_f32_16x16x32_bf16 v[16:19], v[240:243], a[64:67], v[16:19]\n\t"
    "v_perm_b32 v224, v197, v193, s53\n\t"
    "v_perm_b32 v228, v197, v193, s54\n\t"
    "v_mfma_f32_16x16x32_bf16 v[32:35], v[244:247], a[64:67], v[32:35]\n\t"
    "v_perm_b32 v225, v205, v201, s53\n\t"
    "v_perm_b32 v229, v205, v201, s54\n\t"
    "v_mfma_f32_16x16x32_bf16 v[36:39], v[244:247], a[68:71], v[36:39]\n\t"
    "v_perm_b32 v226, v213, v209, s53\n\t"
    "v_perm_b32 v230, v213, v209, s54\n\t"
    "v_mfma_f32_16x16x32_bf16 v[20:23], v[240:243], a[68:71], v[20:23]\n\t"
    "v_perm_b32 v227, v221, v217, s53\n\t"
    "v_perm_b32 v231, v221, v217, s54\n\t"
    "v_mfma_f32_16x16x32_bf16 v[24:27], v[240:243], a[72:75], v[24:27]\n\t"
    "v_mfma_f32_16x16x32_bf16 v[40:43], v[244:247], a[72:75], v[40:43]\n\t"
    "v_mfma_f32_16x16x32_bf16 v[44:47], v[244:247], a[76:79], v[44:47]\n\t"
    "v_mfma_f32_16x16x32_bf16 v[28:31], v[240:243], a[76:79], v[28:31]\n\t"
    "v_mfma_f32_16x16x32_bf16 v[48:51], v[224:227], a[64:67], v[48:51]\n\t"
    "v_perm_b32 v232, v198, v194, s53\n\t"
    "v_perm_b32 v236, v198, v194, s54\n\t"
    "v_mfma_f32_16x16x32_bf16 v[64:67], v[228:231], a[64:67], v[64:67]\n\t"
    "v_perm_b32 v233, v206, v202, s53\n\t"
    "v_perm_b32 v237, v206, v202, s54\n\t"
    "v_mfma_f32_16x16x32_bf16 v[68:71], v[228:231], a[68:71], v[68:71]\n\t"
    "v_perm_b32 v234, v214, v210, s53\n\t"
    "v_perm_b32 v238, v214, v210, s54\n\t"
    "v_mfma_f32_16x16x32_bf16 v[52:55], v[224:227], a[68:71], v[52:55]\n\t"
    "v_perm_b32 v235, v222, v218, s53\n\t"
    "v_perm_b32 v239, v222, v218, s54\n\t"
    "v_mfma_f32_16x16x32_bf16 v[56:59], v[224:227], a[72:75], v[56:59]\n\t"
    "v_mfma_f32_16x16x32_bf16 v[72:75], v[228:231], a[72:75], v[72:75]\n\t"
    "v_mfma_f32_16x16x32_bf16 v[76:79], v[228:231], a[76:79], v[76:79]\n\t"
    "v_mfma_f32_16x16x32_bf16 v[60:63], v[224:227], a[76:79], v[60:63]\n\t"
    "v_mfma_f32_16x16x32_bf16 v[80:83], v[232:235], a[64:67], v[80:83]\n\t"
    "s_waitcnt vmcnt(8)\n\t"
    "v_perm_b32 v240, v132, v128, s53\n\t"
    "v_perm_b32 v244, v132, v128, s54\n\t"
    "v_perm_b32 v241, v140, v136, s53\n\t"
    "v_mfma_f32_16x16x32_bf16 v[96:99], v[236:239], a[64:67], v[96:99]\n\t"
    "v_perm_b32 v245, v140, v136, s54\n\t"
    "v_perm_b32 v242, v148, v144, s53\n\t"
    "v_perm_b32 v246, v148, v144, s54\n\t"
    "v_mfma_f32_16x16x32_bf16 v[100:103], v[236:239], a[68:71], v[100:103]\n\t"
    "v_perm_b32 v243, v156, v152, s53\n\t"
    "v_perm_b32 v247, v156, v152, s54\n\t"
    "s_sub_u32 s55, 239616, s52\n\t"
    "v_mfma_f32_16x16x32_bf16 v[84:87], v[232:235], a[68:71], v[84:87]\n\t"
    "buffer_load_dwordx3 v[192:194], v252, %[sS], s55 offen offset:0\n\t"
    "buffer_load_dwordx3 v[196:198], v252, %[sS], s55 offen offset:832\n\t"
    "buffer_load_dwordx3 v[200:202], v252, %[sS], s55 offen offset:1664\n\t"
    "v_mfma_f32_16x16x32_bf16 v[88:91], v[232:235], a[72:75], v[88:91]\n\t"
    "buffer_load_dwordx3 v[204:206], v252, %[sS], s55 offen offset:2496\n\t"
    "buffer_load_dwordx3 v[208:210], v253, %[sS], s55 offen offset:0\n\t"
    "buffer_load_dwordx3 v[212:214], v253, %[sS], s55 offen offset:832\n\t"
    "v_mfma_f32_16x16x32_bf16 v[104:107], v[236:239], a[72:75], v[104:107]\n\t"
    "buffer_load_dwordx3 v[216:218], v253, %[sS], s55 offen offset:1664\n\t"
    "buffer_load_dwordx3 v[220:222], v253, %[sS], s55 offen offset:2496\n\t"
    "v_mfma_f32_16x16x32_bf16 v[108:111], v[236:239], a[76:79], v[108:111]\n\t"
    "v_mfma_f32_16x16x32_bf16 v[92:95], v[232:235], a[76:79], v[92:95]\n\t"
    "v_mfma_f32_16x16x32_bf16 v[16:19], v[240:243], a[96:99], v[16:19]\n\t"
    "v_perm_b32 v224, v133, v129, s53\n\t"
    "v_perm_b32 v228, v133, v129, s54\n\t"
    "v_mfma_f32_16x16x32_bf16 v[32:35], v[244:247], a[96:99], v[32:35]\n\t"
    "v_perm_b32 v225, v141, v137, s53\n\t"
    "v_perm_b32 v229, v141, v137, s54\n\t"
    "v_mfma_f32_16x16x32_bf16 v[36:39], v[244:247], a[100:103], v[36:39]\n\t"
    "v_perm_b32 v226, v149, v145, s53\n\t"
    "v_perm_b32 v230, v149, v145, s54\n\t"
    "v_mfma_f32_16x16x32_bf16 v[20:23], v[240:243], a[100:103], v[20:23]\n\t"
    "v_perm_b32 v227, v157, v153, s53\n\t"
    "v_perm_b32 v231, v157, v153, s54\n\t"
    "v_mfma_f32_16x16x32_bf16 v[24:27], v[240:243], a[104:107], v[24:27]\n\t"
    "v_mfma_f32_16x16x32_bf16 v[40:43], v[244:247], a[104:107], v[40:43]\n\t"
    "v_mfma_f32_16x16x32_bf16 v[44:47], v[244:247], a[108:111], v[44:47]\n\t"
    "v_mfma_f32_16x16x32_bf16 v[28:31], v[240:243], a[108:111], v[28:31]\n\t"
    "v_mfma_f32_16x16x32_bf16 v[48:51], v[224:227], a[96:99], v[48:51]\n\t"
    "v_perm_b32 v232, v134, v130, s53\n\t"
    "v_perm_b32 v236, v134, v130, s54\n\t"
    "v_mfma_f32_16x16x32_bf16 v[64:67], v[228:231], a[96:99], v[64:67]\n\t"
    "v_perm_b32 v233, v142, v138, s53\n\t"
    "v_perm_b32 v237, v142, v138, s54\n\t"
    "v_mfma_f32_16x16x32_bf16 v[68:71], v[228:231], a[100:103], v[68:71]\n\t"
    "v_perm_b32 v234, v150, v146, s53\n\t"
    "v_perm_b32 v238, v150, v146, s54\n\t"
    "v_mfma_f32_16x16x32_bf16 v[52:55], v[224:227], a[100:103], v[52:55]\n\t"
    "v_perm_b32 v235, v158, v154, s53\n\t"
    "v_perm_b32 v239, v158, v154, s54\n\t"
    "v_mfma_f32_16x16x32_bf16 v[56:59], v[224:227], a[104:107], v[56:59]\n\t"
    "v_mfma_f32_16x16x32_bf16 v[72:75], v[228:231], a[104:107], v[72:75]\n\t"
    "v_mfma_f32_16x16x32_bf16 v[76:79], v[228:231], a[108:111], v[76:79]\n\t"
    "v_mfma_f32_16x16x32_bf16 v[60:63], v[224:227], a[108:111], v[60:63]\n\t"
    "v_mfma_f32_16x16x32_bf16 v[80:83], v[232:235], a[96:99], v[80:83]\n\t"
    "s_waitcnt vmcnt(8)\n\t"
    "v_perm_b32 v240, v164, v160, s53\n\t"
    "v_perm_b32 v244, v164, v160, s54\n\t"
    "v_perm_b32 v241, v172, v168, s53\n\t"
    "v_mfma_f32_16x16x32_bf16 v[96:99], v[236:239], a[96:99], v[96:99]\n\t"
    "v_perm_b32 v245, v172, v168, s54\n\t"
    "v_perm_b32 v242, v180, v176, s53\n\t"
    "v_perm_b32 v246, v180, v176, s54\n\t"
    "v_mfma_f32_16x16x32_bf16 v[100:103], v[236:239], a[100:103], v[100:103]\n\t"
    "v_perm_b32 v243, v188, v184, s53\n\t"
    "v_perm_b32 v247, v188, v184, s54\n\t"
    "s_add_u32 s55, 266240, s52\n\t"
    "v_mfma_f32_16x16x32_bf16 v[84:87], v[232:235], a[100:103], v[84:87]\n\t"
    "buffer_load_dwordx3 v[128:130], v252, %[sS], s55 offen offset:0\n\t"
    "buffer_load_dwordx3 v[132:134], v252, %[sS], s55 offen offset:832\n\t"
    "buffer_load_dwordx3 v[136:138], v252, %[sS], s55 offen offset:1664\n\t"
    "v_mfma_f32_16x16x32_bf16 v[88:91], v[232:235], a[104:107], v[88:91]\n\t"
    "buffer_load_dwordx3 v[140:142], v252, %[sS], s55 offen offset:2496\n\t"
    "buffer_load_dwordx3 v[144:146], v253, %[sS], s55 offen offset:0\n\t"
    "buffer_load_dwordx3 v[148:150], v253, %[sS], s55 offen offset:832\n\t"
    "v_mfma_f32_16x16x32_bf16 v[104:107], v[236:239], a[104:107], v[104:107]\n\t"
    "buffer_load_dwordx3 v[152:154], v253, %[sS], s55 offen offset:1664\n\t"
    "buffer_load_dwordx3 v[156:158], v253, %[sS], s55 offen offset:2496\n\t"
    "v_mfma_f32_16x16x32_bf16 v[108:111], v[236:239], a[108:111], v[108:111]\n\t"
    "v_mfma_f32_16x16x32_bf16 v[92:95], v[232:235], a[108:111], v[92:95]\n\t"
    "v_mfma_f32_16x16x32_bf16 v[16:19], v[240:243], a[112:115], v[16:19]\n\t"
    "v_perm_b32 v224, v165, v161, s53\n\t"
    "v_perm_b32 v228, v165, v161, s54\n\t"
    "v_mfma_f32_16x16x32_bf16 v[32:35], v[244:247], a[112:115], v[32:35]\n\t"
    "v_perm_b32 v225, v173, v169, s53\n\t"
    "v_perm_b32 v229, v173, v169, s54\n\t"
    "v_mfma_f32_16x16x32_bf16 v[36:39], v[244:247], a[116:119], v[36:39]\n\t"
    "v_perm_b32 v226, v181, v177, s53\n\t"
    "v_perm_b32 v230, v181, v177, s54\n\t"
    "v_mfma_f32_16x16x32_bf16 v[20:23], v[240:243], a[116:119], v[20:23]\n\t"
    "v_perm_b32 v227, v189, v185, s53\n\t"
    "v_perm_b32 v231, v189, v185, s54\n\t"
    "v_mfma_f32_16x16x32_bf16 v[24:27], v[240:243], a[120:123], v[24:27]\n\t"
    "v_mfma_f32_16x16x32_bf16 v[40:43], v[244:247], a[120:123], v[40:43]\n\t"
    "v_mfma_f32_16x16x32_bf16 v[44:47], v[244:247], a[124:127], v[44:47]\n\t"
    "v_mfma_f32_16x16x32_bf16 v[28:31], v[240:243], a[124:127], v[28:31]\n\t"
    "v_mfma_f32_16x16x32_bf16 v[48:51], v[224:227], a[112:115], v[48:51]\n\t"
    "v_perm_b32 v232, v166, v162, s53\n\t"
    "v_perm_b32 v236, v166, v162, s54\n\t"
    "v_mfma_f32_16x16x32_bf16 v[64:67], v[228:231], a[112:115], v[64:67]\n\t"
    "v_perm_b32 v233, v174, v170, s53\n\t"
    "v_perm_b32 v237, v174, v170, s54\n\t"
    "v_mfma_f32_16x16x32_bf16 v[68:71], v[228:231], a[116:119], v[68:71]\n\t"
    "v_perm_b32 v234, v182, v178, s53\n\t"
    "v_perm_b32 v238, v182, v178, s54\n\t"
    "v_mfma_f32_16x16x32_bf16 v[52:55], v[224:227], a[116:119], v[52:55]\n\t"
    "v_perm_b32 v235, v190, v186, s53\n\t"
    "v_perm_b32 v239, v190, v186, s54\n\t"
    "v_mfma_f32_16x16x32_bf16 v[56:59], v[224:227], a[120:123], v[56:59]\n\t"
    "v_mfma_f32_16x16x32_bf16 v[72:75], v[228:231], a[120:123], v[72:75]\n\t"
    "v_mfma_f32_16x16x32_bf16 v[76:79], v[228:231], a[124:127], v[76:79]\n\t"
    "v_mfma_f32_16x16x32_bf16 v[60:63], v[224:227], a[124:127], v[60:63]\n\t"
    "v_mfma_f32_16x16x32_bf16 v[80:83], v[232:235], a[112:115], v[80:83]\n\t"
    "s_waitcnt vmcnt(8)\n\t"
    "v_perm_b32 v240, v196, v192, s53\n\t"
    "v_perm_b32 v244, v196, v192, s54\n\t"
    "v_perm_b32 v241, v204, v200, s53\n\t"
    "v_mfma_f32_16x16x32_bf16 v[96:99], v[236:239], a[112:115], v[96:99]\n\t"
    "v_perm_b32 v245, v204, v200, s54\n\t"
    "v_perm_b32 v242, v212, v208, s53\n\t"
    "v_perm_b32 v246, v212, v208, s54\n\t"
    "v_mfma_f32_16x16x32_bf16 v[100:103], v[236:239], a[116:119], v[100:103]\n\t"
    "v_perm_b32 v243, v220, v216, s53\n\t"
    "v_perm_b32 v247, v220, v216, s54\n\t"
    "s_sub_u32 s55, 292864, s52\n\t"
    "v_mfma_f32_16x16x32_bf16 v[84:87], v[232:235], a[116:119], v[84:87]\n\t"
    "buffer_load_dwordx3 v[160:162], v252, %[sS], s55 offen offset:0\n\t"
    "buffer_load_dwordx3 v[164:166], v252, %[sS], s55 offen offset:832\n\t"
    "buffer_load_dwordx3 v[168:170], v252, %[sS], s55 offen offset:1664\n\t"
    "v_mfma_f32_16x16x32_bf16 v[88:91], v[232:235], a[120:123], v[88:91]\n\t"
    "buffer_load_dwordx3 v[172:174], v252, %[sS], s55 offen offset:2496\n\t"
    "buffer_load_dwordx3 v[176:178], v253, %[sS], s55 offen offset:0\n\t"
    "buffer_load_dwordx3 v[180:182], v253, %[sS], s55 offen offset:832\n\t"
    "v_mfma_f32_16x16x32_bf16 v[104:107], v[236:239], a[120:123], v[104:107]\n\t"
    "buffer_load_dwordx3 v[184:186], v253, %[sS], s55 offen offset:1664\n\t"
    "buffer_load_dwordx3 v[188:190], v253, %[sS], s55 offen offset:2496\n\t"
    "v_mfma_f32_16x16x32_bf16 v[108:111], v[236:239], a[124:127], v[108:111]\n\t"
    "v_mfma_f32_16x16x32_bf16 v[92:95], v[232:235], a[124:127], v[92:95]\n\t"
    "v_mfma_f32_16x16x32_bf16 v[16:19], v[240:243], a[144:147], v[16:19]\n\t"
    "v_perm_b32 v224, v197, v193, s53\n\t"
    "v_perm_b32 v228, v197, v193, s54\n\t"
    "v_mfma_f32_16x16x32_bf16 v[32:35], v[244:247], a[144:147], v[32:35]\n\t"
    "v_perm_b32 v225, v205, v201, s53\n\t"
    "v_perm_b32 v229, v205, v201, s54\n\t"
    "v_mfma_f32_16x16x32_bf16 v[36:39], v[244:247], a[148:151], v[36:39]\n\t"
    "v_perm_b32 v226, v213, v209, s53\n\t"
    "v_perm_b32 v230, v213, v209, s54\n\t"
    "v_mfma_f32_16x16x32_bf16 v[20:23], v[240:243], a[148:151], v[20:23]\n\t"
    "v_perm_b32 v227, v221, v217, s53\n\t"
    "v_perm_b32 v231, v221, v217, s54\n\t"
    "s_waitcnt lgkmcnt(14)\n\t"
    "v_mfma_f32_16x16x32_bf16 v[24:27], v[240:243], a[152:155], v[24:27]\n\t"
    "v_mfma_f32_16x16x32_bf16 v[40:43], v[244:247], a[152:155], v[40:43]\n\t"
    "s_waitcnt lgkmcnt(13)\n\t"
    "v_mfma_f32_16x16x32_bf16 v[44:47], v[244:247], a[156:159], v[44:47]\n\t"
    "v_mfma_f32_16x16x32_bf16 v[28:31], v[240:243], a[156:159], v[28:31]\n\t"
    "v_mfma_f32_16x16x32_bf16 v[48:51], v[224:227], a[144:147], v[48:51]\n\t"
    "v_perm_b32 v232, v198, v194, s53\n\t"
    "v_perm_b32 v236, v198, v194, s54\n\t"
    "v_mfma_f32_16x16x32_bf16 v[64:67], v[228:231], a[144:147], v[64:67]\n\t"
    "v_perm_b32 v233, v206, v202, s53\n\t"
    "v_perm_b32 v237, v206, v202, s54\n\t"
    "v_mfma_f32_16x16x32_bf16 v[68:71], v[228:231], a[148:151], v[68:71]\n\t"
    "v_perm_b32 v234, v214, v210, s53\n\t"
    "v_perm_b32 v238, v214, v210, s54\n\t"
    "v_mfma_f32_16x16x32_bf16 v[52:55], v[224:227], a[148:151], v[52:55]\n\t"
    "v_perm_b32 v235, v222, v218, s53\n\t"
    "v_perm_b32 v239, v222, v218, s54\n\t"
    "v_mfma_f32_16x16x32_bf16 v[56:59], v[224:227], a[152:155], v[56:59]\n\t"
    "v_mfma_f32_16x16x32_bf16 v[72:75], v[228:231], a[152:155], v[72:75]\n\t"
    "v_mfma_f32_16x16x32_bf16 v[76:79], v[228:231], a[156:159], v[76:79]\n\t"
    "v_mfma_f32_16x16x32_bf16 v[60:63], v[224:227], a[156:159], v[60:63]\n\t"
    "v_mfma_f32_16x16x32_bf16 v[80:83], v[232:235], a[144:147], v[80:83]\n\t"
    "s_waitcnt vmcnt(8)\n\t"
    "v_perm_b32 v240, v132, v128, s53\n\t"
    "v_perm_b32 v244, v132, v128, s54\n\t"
    "v_perm_b32 v241, v140, v136, s53\n\t"
    "v_mfma_f32_16x16x32_bf16 v[96:99], v[236:239], a[144:147], v[96:99]\n\t"
    "v_perm_b32 v245, v140, v136, s54\n\t"
    "v_perm_b32 v242, v148, v144, s53\n\t"
    "v_perm_b32 v246, v148, v144, s54\n\t"
    "v_mfma_f32_16x16x32_bf16 v[100:103], v[236:239], a[148:151], v[100:103]\n\t"
    "v_perm_b32 v243, v156, v152, s53\n\t"
    "v_perm_b32 v247, v156, v152, s54\n\t"
    "s_mov_b32 s55, 319488\n\t"
    "v_mfma_f32_16x16x32_bf16 v[84:87], v[232:235], a[148:151], v[84:87]\n\t"
    "buffer_load_dwordx3 v[192:194], v252, %[sS], s55 offen offset:0\n\t"
    "buffer_load_dwordx3 v[196:198], v252, %[sS], s55 offen offset:832\n\t"
    "buffer_load_dwordx3 v[200:202], v252, %[sS], s55 offen offset:1664\n\t"
    "v_mfma_f32_16x16x32_bf16 v[88:91], v[232:235], a[152:155], v[88:91]\n\t"
    "buffer_load_dwordx3 v[204:206], v252, %[sS], s55 offen offset:2496\n\t"
    "buffer_load_dwordx3 v[208:210], v253, %[sS], s55 offen offset:0\n\t"
    "buffer_load_dwordx3 v[212:214], v253, %[sS], s55 offen offset:832\n\t"
    "v_mfma_f32_16x16x32_bf16 v[104:107], v[236:239], a[152:155], v[104:107]\n\t"
    "buffer_load_dwordx3 v[216:218], v253, %[sS], s55 offen offset:1664\n\t"
    "buffer_load_dwordx3 v[220:222], v253, %[sS], s55 offen offset:2496\n\t"
    "v_mfma_f32_16x16x32_bf16 v[108:111], v[236:239], a[156:159], v[108:111]\n\t"
    "v_mfma_f32_16x16x32_bf16 v[92:95], v[232:235], a[156:159], v[92:95]\n\t"
    "s_waitcnt lgkmcnt(12)\n\t"
    "v_mfma_f32_16x16x32_bf16 v[16:19], v[240:243], a[160:163], v[16:19]\n\t"
    "v_perm_b32 v224, v133, v129, s53\n\t"
    "v_perm_b32 v228, v133, v129, s54\n\t"
    "v_mfma_f32_16x16x32_bf16 v[32:35], v[244:247], a[160:163], v[32:35]\n\t"
    "v_perm_b32 v225, v141, v137, s53\n\t"
    "v_perm_b32 v229, v141, v137, s54\n\t"
    "s_waitcnt lgkmcnt(11)\n\t"
    "v_mfma_f32_16x16x32_bf16 v[36:39], v[244:247], a[164:167], v[36:39]\n\t"
    "v_perm_b32 v226, v149, v145, s53\n\t"
    "v_perm_b32 v230, v149, v145, s54\n\t"
    "v_mfma_f32_16x16x32_bf16 v[20:23], v[240:243], a[164:167], v[20:23]\n\t"
    "v_perm_b32 v227, v157, v153, s53\n\t"
    "v_perm_b32 v231, v157, v153, s54\n\t"
    "s_waitcnt lgkmcnt(10)\n\t"
    "v_mfma_f32_16x16x32_bf16 v[24:27], v[240:243], a[168:171], v[24:27]\n\t"
    "v_mfma_f32_16x16x32_bf16 v[40:43], v[244:247], a[168:171], v[40:43]\n\t"
    "s_waitcnt lgkmcnt(9)\n\t"
    "v_mfma_f32_16x16x32_bf16 v[44:47], v[244:247], a[172:175], v[44:47]\n\t"
    "v_mfma_f32_16x16x32_bf16 v[28:31], v[240:243], a[172:175], v[28:31]\n\t"
    "v_mfma_f32_16x16x32_bf16 v[48:51], v[224:227], a[160:163], v[48:51]\n\t"
    "v_perm_b32 v232, v134, v130, s53\n\t"
    "v_perm_b32 v236, v134, v130, s54\n\t"
    "v_mfma_f32_16x16x32_bf16 v[64:67], v[228:231], a[160:163], v[64:67]\n\t"
    "v_perm_b32 v233, v142, v138, s53\n\t"
    "v_perm_b32 v237, v142, v138, s54\n\t"
    "v_mfma_f32_16x16x32_bf16 v[68:71], v[228:231], a[164:167], v[68:71]\n\t"
    "v_perm_b32 v234, v150, v146, s53\n\t"
    "v_perm_b32 v238, v150, v146, s54\n\t"
    "v_mfma_f32_16x16x32_bf16 v[52:55], v[224:227], a[164:167], v[52:55]\n\t"
    "v_perm_b32 v235, v158, v154, s53\n\t"
    "v_perm_b32 v239, v158, v154, s54\n\t"
    "v_mfma_f32_16x16x32_bf16 v[56:59], v[224:227], a[168:171], v[56:59]\n\t"
    "v_mfma_f32_16x16x32_bf16 v[72:75], v[228:231], a[168:171], v[72:75]\n\t"
    "v_mfma_f32_16x16x32_bf16 v[76:79], v[228:231], a[172:175], v[76:79]\n\t"
    "v_mfma_f32_16x16x32_bf16 v[60:63], v[224:227], a[172:175], v[60:63]\n\t"
    "v_mfma_f32_16x16x32_bf16 v[80:83], v[232:235], a[160:163], v[80:83]\n\t"
    "s_waitcnt vmcnt(8)\n\t"
    "v_perm_b32 v240, v164, v160, s53\n\t"
    "v_perm_b32 v244, v164, v160, s54\n\t"
    "v_mfma_f32_16x16x32_bf16 v[96:99], v[236:239], a[160:163], v[96:99]\n\t"
    "v_perm_b32 v241, v172, v168, s53\n\t"
    "v_perm_b32 v245, v172, v168, s54\n\t"
    "v_mfma_f32_16x16x32_bf16 v[100:103], v[236:239], a[164:167], v[100:103]\n\t"
    "v_perm_b32 v242, v180, v176, s53\n\t"
    "v_perm_b32 v246, v180, v176, s54\n\t"
    "v_mbcnt_lo_u32_b32 v12, -1, 0\n\t"
    "v_mbcnt_hi_u32_b32 v12, -1, v12\n\t"
    "v_mfma_f32_16x16x32_bf16 v[84:87], v[232:235], a[164:167], v[84:87]\n\t"
    "v_perm_b32 v243, v188, v184, s53\n\t"
    "v_perm_b32 v247, v188, v184, s54\n\t"
    "v_and_b32_e32 v13, 1, v12\n\t"
    "v_lshrrev_b32_e32 v12, 1, v12\n\t"
    "v_mfma_f32_16x16x32_bf16 v[88:91], v[232:235], a[168:171], v[88:91]\n\t"
    "v_lshlrev_b32_e32 v12, 7, v12\n\t"
    "v_lshl_or_b32 v12, v13, 4, v12\n\t"
    "v_mfma_f32_16x16x32_bf16 v[104:107], v[236:239], a[168:171], v[104:107]\n\t"
    "s_mov_b32 s42, 2121856\n\t"
    "buffer_load_dwordx4 v[12:15], v12, %[sW], s42 offen sc1\n\t"
    "v_mfma_f32_16x16x32_bf16 v[108:111], v[236:239], a[172:175], v[108:111]\n\t"
    "s_movk_i32 s45, 8192\n\t"
    "v_mfma_f32_16x16x32_bf16 v[92:95], v[232:235], a[172:175], v[92:95]\n\t"
    "s_waitcnt lgkmcnt(8)\n\t"
    "v_mfma_f32_16x16x32_bf16 v[16:19], v[240:243], a[176:179], v[16:19]\n\t"
    "v_perm_b32 v224, v165, v161, s53\n\t"
    "v_perm_b32 v228, v165, v161, s54\n\t"
    "v_mfma_f32_16x16x32_bf16 v[32:35], v[244:247], a[176:179], v[32:35]\n\t"
    "v_perm_b32 v225, v173, v169, s53\n\t"
    "v_perm_b32 v229, v173, v169, s54\n\t"
    "s_waitcnt lgkmcnt(7)\n\t"
    "v_mfma_f32_16x16x32_bf16 v[36:39], v[244:247], a[180:183], v[36:39]\n\t"
    "v_perm_b32 v226, v181, v177, s53\n\t"
    "v_perm_b32 v230, v181, v177, s54\n\t"
    "v_mfma_f32_16x16x32_bf16 v[20:23], v[240:243], a[180:183], v[20:23]\n\t"
    "v_perm_b32 v227, v189, v185, s53\n\t"
    "v_perm_b32 v231, v189, v185, s54\n\t"
    "s_waitcnt lgkmcnt(6)\n\t"
    "v_mfma_f32_16x16x32_bf16 v[24:27], v[240:243], a[184:187], v[24:27]\n\t"
    "v_mfma_f32_16x16x32_bf16 v[40:43], v[244:247], a[184:187], v[40:43]\n\t"
    "s_waitcnt lgkmcnt(5)\n\t"
    "v_mfma_f32_16x16x32_bf16 v[44:47], v[244:247], a[188:191], v[44:47]\n\t"
    "v_mfma_f32_16x16x32_bf16 v[28:31], v[240:243], a[188:191], v[28:31]\n\t"
    "v_mfma_f32_16x16x32_bf16 v[48:51], v[224:227], a[176:179], v[48:51]\n\t"
    "v_perm_b32 v232, v166, v162, s53\n\t"
    "v_perm_b32 v236, v166, v162, s54\n\t"
    "v_mfma_f32_16x16x32_bf16 v[64:67], v[228:231], a[176:179], v[64:67]\n\t"
    "v_perm_b32 v233, v174, v170, s53\n\t"
    "v_perm_b32 v237, v174, v170, s54\n\t"
    "v_mfma_f32_16x16x32_bf16 v[68:71], v[228:231], a[180:183], v[68:71]\n\t"
    "v_perm_b32 v234, v182, v178, s53\n\t"
    "v_perm_b32 v238, v182, v178, s54\n\t"
    "v_mfma_f32_16x16x32_bf16 v[52:55], v[224:227], a[180:183], v[52:55]\n\t"
    "v_perm_b32 v235, v190, v186, s53\n\t"
    "v_perm_b32 v239, v190, v186, s54\n\t"
    "v_mfma_f32_16x16x32_bf16 v[56:59], v[224:227], a[184:187], v[56:59]\n\t"
    "v_mfma_f32_16x16x32_bf16 v[72:75], v[228:231], a[184:187], v[72:75]\n\t"
    "v_mfma_f32_16x16x32_bf16 v[76:79], v[228:231], a[188:191], v[76:79]\n\t"
    "v_mfma_f32_16x16x32_bf16 v[60:63], v[224:227], a[188:191], v[60:63]\n\t"
    "v_mfma_f32_16x16x32_bf16 v[80:83], v[232:235], a[176:179], v[80:83]\n\t"
    "s_waitcnt vmcnt(1)\n\t"
    "v_perm_b32 v240, v196, v192, s53\n\t"
    "v_perm_b32 v244, v196, v192, s54\n\t"
    "v_mfma_f32_16x16x32_bf16 v[96:99], v[236:239], a[176:179], v[96:99]\n\t"
    "v_perm_b32 v241, v204, v200, s53\n\t"
    "v_perm_b32 v245, v204, v200, s54\n\t"
    "v_mfma_f32_16x16x32_bf16 v[100:103], v[236:239], a[180:183], v[100:103]\n\t"
    "v_perm_b32 v242, v212, v208, s53\n\t"
    "v_perm_b32 v246, v212, v208, s54\n\t"
    "v_mfma_f32_16x16x32_bf16 v[84:87], v[232:235], a[180:183], v[84:87]\n\t"
    "v_perm_b32 v243, v220, v216, s53\n\t"
    "v_perm_b32 v247, v220, v216, s54\n\t"
    "v_mfma_f32_16x16x32_bf16 v[88:91], v[232:235], a[184:187], v[88:91]\n\t"
    "v_mfma_f32_16x16x32_bf16 v[104:107], v[236:239], a[184:187], v[104:107]\n\t"
    "v_mfma_f32_16x16x32_bf16 v[108:111], v[236:239], a[188:191], v[108:111]\n\t"
    "v_mfma_f32_16x16x32_bf16 v[92:95], v[232:235], a[188:191], v[92:95]\n\t"
    "s_waitcnt lgkmcnt(4)\n\t"
    "v_mfma_f32_16x16x32_bf16 v[16:19], v[240:243], a[192:195], v[16:19]\n\t"
    "v_perm_b32 v224, v197, v193, s53\n\t"
    "v_perm_b32 v228, v197, v193, s54\n\t"
    "v_perm_b32 v225, v205, v201, s53\n\t"
    "v_perm_b32 v229, v205, v201, s54\n\t"
    "v_mfma_f32_16x16x32_bf16 v[32:35], v[244:247], a[192:195], v[32:35]\n\t"
    "v_perm_b32 v226, v213, v209, s53\n\t"
    "v_perm_b32 v230, v213, v209, s54\n\t"
    "v_perm_b32 v227, v221, v217, s53\n\t"
    "v_perm_b32 v231, v221, v217, s54\n\t"
    "s_waitcnt lgkmcnt(3)\n\t"
    "v_mfma_f32_16x16x32_bf16 v[36:39], v[244:247], a[196:199], v[36:39]\n\t"
    "v_perm_b32 v232, v198, v194, s53\n\t"
    "v_perm_b32 v236, v198, v194, s54\n\t"
    "v_perm_b32 v233, v206, v202, s53\n\t"
    "v_perm_b32 v237, v206, v202, s54\n\t"
    "v_mfma_f32_16x16x32_bf16 v[20:23], v[240:243], a[196:199], v[20:23]\n\t"
    "v_perm_b32 v234, v214, v210, s53\n\t"
    "v_perm_b32 v238, v214, v210, s54\n\t"
    "v_perm_b32 v235, v222, v218, s53\n\t"
    "v_perm_b32 v239, v222, v218, s54\n\t"
    "s_waitcnt lgkmcnt(2)\n\t"
    "v_mfma_f32_16x16x32_bf16 v[24:27], v[240:243], a[200:203], v[24:27]\n\t"
    "v_accvgpr_write_b32 a32, v232\n\t"
    "v_accvgpr_write_b32 a33, v233\n\t"
    "v_accvgpr_write_b32 a34, v234\n\t"
    "v_accvgpr_write_b32 a35, v235\n\t"
    "v_mfma_f32_16x16x32_bf16 v[40:43], v[244:247], a[200:203], v[40:43]\n\t"
    "v_accvgpr_write_b32 a36, v236\n\t"
    "v_accvgpr_write_b32 a37, v237\n\t"
    "v_accvgpr_write_b32 a38, v238\n\t"
    "v_accvgpr_write_b32 a39, v239\n\t"
    "s_waitcnt lgkmcnt(1)\n\t"
    "v_mfma_f32_16x16x32_bf16 v[44:47], v[244:247], a[204:207], v[44:47]\n\t"
    "v_mfma_f32_16x16x32_bf16 v[28:31], v[240:243], a[204:207], v[28:31]\n\t"
    "v_mfma_f32_16x16x32_bf16 v[48:51], v[224:227], a[192:195], v[48:51]\n\t"
    "ds_read_b128 v[140:143], v0 offset:0\n\t"
    "ds_read_b128 v[156:159], v254 offset:0\n\t"
    "ds_read_b128 v[172:175], v255 offset:0\n\t"
    "v_mfma_f32_16x16x32_bf16 v[64:67], v[228:231], a[192:195], v[64:67]\n\t"
    "ds_read_b128 v[188:191], v0 offset:1024\n\t"
    "ds_read_b128 v[204:207], v254 offset:1024\n\t"
    "ds_read_b128 v[220:223], v255 offset:1024\n\t"
    "v_mfma_f32_16x16x32_bf16 v[68:71], v[228:231], a[196:199], v[68:71]\n\t"
    "ds_read_b128 v[232:235], v0 offset:2048\n\t"
    "ds_read_b128 v[236:239], v254 offset:2048\n\t"
    "ds_read_b128 v[240:243], v255 offset:2048\n\t"
    "v_mfma_f32_16x16x32_bf16 v[52:55], v[224:227], a[196:199], v[52:55]\n\t"
    "ds_read_b128 v[244:247], v0 offset:3072\n\t"
    "ds_read_b128 v[248:251], v254 offset:3072\n\t"
    "v_mfma_f32_16x16x32_bf16 v[56:59], v[224:227], a[200:203], v[56:59]\n\t"
    "v_mfma_f32_16x16x32_bf16 v[72:75], v[228:231], a[200:203], v[72:75]\n\t"
    "v_mfma_f32_16x16x32_bf16 v[76:79], v[228:231], a[204:207], v[76:79]\n\t"
    "v_mfma_f32_16x16x32_bf16 v[60:63], v[224:227], a[204:207], v[60:63]\n\t"
    "v_mfma_f32_16x16x32_bf16 v[80:83], a[32:35], a[192:195], v[80:83]\n\t"
    "v_mfma_f32_16x16x32_bf16 v[96:99], a[36:39], a[192:195], v[96:99]\n\t"
    "v_mfma_f32_16x16x32_bf16 v[100:103], a[36:39], a[196:199], v[100:103]\n\t"
    "v_mfma_f32_16x16x32_bf16 v[84:87], a[32:35], a[196:199], v[84:87]\n\t"
    "v_mfma_f32_16x16x32_bf16 v[88:91], a[32:35], a[200:203], v[88:91]\n\t"
    "s_waitcnt vmcnt(0)\n\t"
    "s_nop 0\n\t"
    "64:\n\tv_xor_b32_e32 v12, s51, v12\n\tv_xor_b32_e32 v13, s51, v13\n\tv_xor_b32_e32 v14, s51, v14\n\tv_xor_b32_e32 v15, s51, v15\n\tv_or3_b32 v12, v12, v13, v14\n\tv_or_b32_e32 v12, v12, v15\n\tv_cmp_ne_u32_e32 vcc, 0, v12\n\ts_nop 4\n\ts_cmp_lg_u64 vcc, 0\n\ts_cbranch_scc1 62f\n\t63:\n\t"
    "v_mfma_f32_16x16x32_bf16 v[104:107], a[36:39], a[200:203], v[104:107]\n\t"
    "s_mov_b32 s40, 16384\n\t"
    "buffer_load_dwordx4 a[40:43], v1, %[sW], s40 offen sc1\n\t"
    "v_mfma_f32_16x16x32_bf16 v[108:111], a[36:39], a[204:207], v[108:111]\n\t"
    "s_mov_b32 s40, 17408\n\t"
    "buffer_load_dwordx4 a[44:47], v1, %[sW], s40 offen sc1\n\t"
    "ds_read_b128 v[12:15], v255 offset:3072\n\t"
    "v_mfma_f32_16x16x32_bf16 v[92:95], a[32:35], a[204:207], v[92:95]\n\t"
    "s_mov_b32 s40, 18432\n\t"
    "buffer_load_dwordx4 a[80:83], v1, %[sW], s40 offen sc1\n\t"
    "s_waitcnt lgkmcnt(9)\n\t"
    "v_lshlrev_b32_e32 v128, 16, v140\n\t"
    "s_mov_b32 s40, 19456\n\t"
    "buffer_load_dwordx4 a[84:87], v1, %[sW], s40 offen sc1\n\t"
    "s_mov_b32 s40, 30720\n\t"
    "buffer_load_dwordx4 a[88:91], v1, %[sW], s40 offen sc1\n\t"
    "s_mov_b32 s40, 31744\n\t"
    "buffer_load_dwordx4 a[92:95], v1, %[sW], s40 offen sc1\n\t"
    "s_mov_b32 s40, 32768\n\t"
    "buffer_load_dwordx4 a[128:131], v1, %[sW], s40 offen sc1\n\t"
    "s_mov_b32 s40, 87040\n\t"
    "buffer_load_dword a252, v11, %[sW], s40 offen sc1\n\t"
    "v_add_u32_e32 v11, 22528, v11\n\t"
    "buffer_load_dwordx4 a[208:211], v1, %[sW], s40 offen offset:0 sc1\n\t"
    "buffer_load_dwordx4 a[212:215], v1, %[sW], s40 offen offset:1024 sc1\n\t"
    "buffer_load_dwordx4 a[216:219], v1, %[sW], s40 offen offset:2048 sc1\n\t"
    "buffer_load_dwordx4 a[220:223], v1, %[sW], s40 offen offset:3072 sc1\n\t"
    "s_add_u32 s40, s40, 4096\n\t"
    "buffer_load_dwordx4 a[224:227], v1, %[sW], s40 offen offset:0 sc1\n\t"
    "buffer_load_dwordx4 a[228:231], v1, %[sW], s40 offen offset:1024 sc1\n\t"
    "buffer_load_dwordx4 a[232:235], v1, %[sW], s40 offen offset:2048 sc1\n\t"
    "buffer_load_dwordx4 a[236:239], v1, %[sW], s40 offen offset:3072 sc1\n\t"
    "s_add_u32 s40, s40, 4096\n\t"
    "buffer_load_dwordx4 a[240:243], v1, %[sW], s40 offen offset:0 sc1\n\t"
    "buffer_load_dwordx4 a[244:247], v1, %[sW], s40 offen offset:1024 sc1\n\t"
    "s_mov_b32 s40, 97280\n\t"
    "v_and_b32_e32 v144, 0xffff0000, v140\n\t"
    "v_lshlrev_b32_e32 v160, 16, v141\n\t"
    "v_and_b32_e32 v176, 0xffff0000, v141\n\t"
    "v_lshlrev_b32_e32 v192, 16, v142\n\t"
    "v_and_b32_e32 v208, 0xffff0000, v142\n\t"
    "v_lshlrev_b32_e32 v129, 16, v143\n\t"
    "v_and_b32_e32 v145, 0xffff0000, v143\n\t"
    "v_lshlrev_b32_e32 v161, 16, v156\n\t"
    "v_and_b32_e32 v177, 0xffff0000, v156\n\t"
    "v_lshlrev_b32_e32 v193, 16, v157\n\t"
    "v_and_b32_e32 v209, 0xffff0000, v157\n\t"
    "v_lshlrev_b32_e32 v130, 16, v158\n\t"
    "v_and_b32_e32 v146, 0xffff0000, v158\n\t"
    "v_lshlrev_b32_e32 v162, 16, v159\n\t"
    "v_and_b32_e32 v178, 0xffff0000, v159\n\t"
    "v_lshlrev_b32_e32 v194, 16, v172\n\t"
    "v_and_b32_e32 v210, 0xffff0000, v172\n\t"
    "v_lshlrev_b32_e32 v131, 16, v173\n\t"
    "v_and_b32_e32 v147, 0xffff0000, v173\n\t"
    "v_lshlrev_b32_e32 v163, 16, v174\n\t"
    "v_and_b32_e32 v179, 0xffff0000, v174\n\t"
    "v_lshlrev_b32_e32 v195, 16, v175\n\t"
    "v_and_b32_e32 v211, 0xffff0000, v175\n\t"
    "s_waitcnt lgkmcnt(6)\n\t"
    "v_lshlrev_b32_e32 v132, 16, v188\n\t"
    "v_and_b32_e32 v148, 0xffff0000, v188\n\t"
    "v_lshlrev_b32_e32 v164, 16, v189\n\t"
    "v_and_b32_e32 v180, 0xffff0000, v189\n\t"
    "v_lshlrev_b32_e32 v196, 16, v190\n\t"
    "v_and_b32_e32 v212, 0xffff0000, v190\n\t"
    "v_lshlrev_b32_e32 v133, 16, v191\n\t"
    "v_and_b32_e32 v149, 0xffff0000, v191\n\t"
    "v_lshlrev_b32_e32 v165, 16, v204\n\t"
    "v_and_b32_e32 v181, 0xffff0000, v204\n\t"
    "v_lshlrev_b32_e32 v197, 16, v205\n\t"
    "v_and_b32_e32 v213, 0xffff0000, v205\n\t"
    "v_lshlrev_b32_e32 v134, 16, v206\n\t"
    "v_and_b32_e32 v150, 0xffff0000, v206\n\t"
    "v_lshlrev_b32_e32 v166, 16, v207\n\t"
    "v_and_b32_e32 v182, 0xffff0000, v207\n\t"
    "v_lshlrev_b32_e32 v198, 16, v220\n\t"
    "v_and_b32_e32 v214, 0xffff0000, v220\n\t"
    "v_lshlrev_b32_e32 v135, 16, v221\n\t"
    "v_and_b32_e32 v151, 0xffff0000, v221\n\t"
    "v_lshlrev_b32_e32 v167, 16, v222\n\t"
    "v_and_b32_e32 v183, 0xffff0000, v222\n\t"
    "v_lshlrev_b32_e32 v199, 16, v223\n\t"
    "v_and_b32_e32 v215, 0xffff0000, v223\n\t"
    "s_waitcnt lgkmcnt(3)\n\t"
    "v_lshlrev_b32_e32 v136, 16, v232\n\t"
    "v_and_b32_e32 v152, 0xffff0000, v232\n\t"
    "v_lshlrev_b32_e32 v168, 16, v233\n\t"
    "v_and_b32_e32 v184, 0xffff0000, v233\n\t"
    "v_lshlrev_b32_e32 v200, 16, v234\n\t"
    "v_and_b32_e32 v216, 0xffff0000, v234\n\t"
    "v_lshlrev_b32_e32 v137, 16, v235\n\t"
    "v_and_b32_e32 v153, 0xffff0000, v235\n\t"
    "v_lshlrev_b32_e32 v169, 16, v236\n\t"
    "v_and_b32_e32 v185, 0xffff0000, v236\n\t"
    "v_lshlrev_b32_e32 v201, 16, v237\n\t"
    "v_and_b32_e32 v217, 0xffff0000, v237\n\t"
    "v_lshlrev_b32_e32 v138, 16, v238\n\t"
    "v_and_b32_e32 v154, 0xffff0000, v238\n\t"
    "v_lshlrev_b32_e32 v170, 16, v239\n\t"
    "v_and_b32_e32 v186, 0xffff0000, v239\n\t"
    "v_lshlrev_b32_e32 v202, 16, v240\n\t"
    "v_and_b32_e32 v218, 0xffff0000, v240\n\t"
    "v_lshlrev_b32_e32 v139, 16, v241\n\t"
    "v_and_b32_e32 v155, 0xffff0000, v241\n\t"
    "v_lshlrev_b32_e32 v171, 16, v242\n\t"
    "v_and_b32_e32 v187, 0xffff0000, v242\n\t"
    "v_lshlrev_b32_e32 v203, 16, v243\n\t"
    "v_and_b32_e32 v219, 0xffff0000, v243\n\t"
    "s_waitcnt lgkmcnt(0)\n\t"
    "v_lshlrev_b32_e32 v140, 16, v244\n\t"
    "v_and_b32_e32 v156, 0xffff0000, v244\n\t"
    "v_lshlrev_b32_e32 v172, 16, v245\n\t"
    "v_and_b32_e32 v188, 0xffff0000, v245\n\t"
    "v_lshlrev_b32_e32 v204, 16, v246\n\t"
    "v_and_b32_e32 v220, 0xffff0000, v246\n\t"
    "v_lshlrev_b32_e32 v141, 16, v247\n\t"
    "v_and_b32_e32 v157, 0xffff0000, v247\n\t"
    "v_lshlrev_b32_e32 v173, 16, v248\n\t"
    "v_and_b32_e32 v189, 0xffff0000, v248\n\t"
    "v_lshlrev_b32_e32 v205, 16, v249\n\t"
    "v_and_b32_e32 v221, 0xffff0000, v249\n\t"
    "v_lshlrev_b32_e32 v142, 16, v250\n\t"
    "v_and_b32_e32 v158, 0xffff0000, v250\n\t"
    "v_lshlrev_b32_e32 v174, 16, v251\n\t"
    "v_and_b32_e32 v190, 0xffff0000, v251\n\t"
    "v_lshlrev_b32_e32 v206, 16, v12\n\t"
    "v_and_b32_e32 v222, 0xffff0000, v12\n\t"
    "v_lshlrev_b32_e32 v143, 16, v13\n\t"
    "v_and_b32_e32 v159, 0xffff0000, v13\n\t"
    "v_lshlrev_b32_e32 v175, 16, v14\n\t"
    "v_and_b32_e32 v191, 0xffff0000, v14\n\t"
    "v_lshlrev_b32_e32 v207, 16, v15\n\t"
    "v_and_b32_e32 v223, 0xffff0000, v15\n\t"
    "ds_read_b128 v[232:235], v10 offset:0\n\t"
    "ds_read_b128 v[236:239], v10 offset:64\n\t"
    "s_waitcnt lgkmcnt(1)\n\t"
    "v_pk_add_f32 v[240:241], v[16:17], v[232:233]\n\t"
    "v_pk_add_f32 v[242:243], v[18:19], v[234:235]\n\t"
    "s_waitcnt lgkmcnt(0)\n\t"
    "v_pk_add_f32 v[248:249], v[32:33], v[236:237]\n\t"
    "v_pk_add_f32 v[250:251], v[34:35], v[238:239]\n\t"
    "v_pk_mul_f32 v[240:241], v[240:241], v[128:129]\n\t"
    "v_pk_mul_f32 v[242:243], v[242:243], v[130:131]\n\t"
    "v_pk_mul_f32 v[248:249], v[248:249], v[144:145]\n\t"
    "v_pk_mul_f32 v[250:251], v[250:251], v[146:147]\n\t"
    "v_cvt_pk_bf16_f32 v240, v240, v248\n\t"
    "v_cvt_pk_bf16_f32 v241, v241, v249\n\t"
    "v_cvt_pk_bf16_f32 v242, v242, v250\n\t"
    "v_cvt_pk_bf16_f32 v243, v243, v251\n\t"
    "ds_write_b128 v2, v[240:243] offset:0\n\t"
    "v_pk_add_f32 v[244:245], v[20:21], v[232:233]\n\t"
    "v_pk_add_f32 v[246:247], v[22:23], v[234:235]\n\t"
    "v_pk_add_f32 v[248:249], v[36:37], v[236:237]\n\t"
    "v_pk_add_f32 v[250:251], v[38:39], v[238:239]\n\t"
    "v_pk_mul_f32 v[244:245], v[244:245], v[132:133]\n\t"
    "v_pk_mul_f32 v[246:247], v[246:247], v[134:135]\n\t"
    "v_pk_mul_f32 v[248:249], v[248:249], v[148:149]\n\t"
    "v_pk_mul_f32 v[250:251], v[250:251], v[150:151]\n\t"
    "v_cvt_pk_bf16_f32 v244, v244, v248\n\t"
    "v_cvt_pk_bf16_f32 v245, v245, v249\n\t"
    "v_cvt_pk_bf16_f32 v246, v246, v250\n\t"
    "v_cvt_pk_bf16_f32 v247, v247, v251\n\t"
    "ds_write_b128 v2, v[244:247] offset:1024\n\t"
    "v_pk_add_f32 v[240:241], v[24:25], v[232:233]\n\t"
    "v_pk_add_f32 v[242:243], v[26:27], v[234:235]\n\t"
    "v_pk_add_f32 v[248:249], v[40:41], v[236:237]\n\t"
    "v_pk_add_f32 v[250:251], v[42:43], v[238:239]\n\t"
    "v_pk_mul_f32 v[240:241], v[240:241], v[136:137]\n\t"
    "v_pk_mul_f32 v[242:243], v[242:243], v[138:139]\n\t"
    "v_pk_mul_f32 v[248:249], v[248:249], v[152:153]\n\t"
    "v_pk_mul_f32 v[250:251], v[250:251], v[154:155]\n\t"
    "v_cvt_pk_bf16_f32 v240, v240, v248\n\t"
    "v_cvt_pk_bf16_f32 v241, v241, v249\n\t"
    "v_cvt_pk_bf16_f32 v242, v242, v250\n\t"
    "v_cvt_pk_bf16_f32 v243, v243, v251\n\t"
    "ds_write_b128 v2, v[240:243] offset:2048\n\t"
    "v_pk_add_f32 v[244:245], v[28:29], v[232:233]\n\t"
    "v_pk_add_f32 v[246:247], v[30:31], v[234:235]\n\t"
    "v_pk_add_f32 v[248:249], v[44:45], v[236:237]\n\t"
    "v_pk_add_f32 v[250:251], v[46:47], v[238:239]\n\t"
    "v_pk_mul_f32 v[244:245], v[244:245], v[140:141]\n\t"
    "v_pk_mul_f32 v[246:247], v[246:247], v[142:143]\n\t"
    "v_pk_mul_f32 v[248:249], v[248:249], v[156:157]\n\t"
    "v_pk_mul_f32 v[250:251], v[250:251], v[158:159]\n\t"
    "v_cvt_pk_bf16_f32 v244, v244, v248\n\t"
    "v_cvt_pk_bf16_f32 v245, v245, v249\n\t"
    "v_cvt_pk_bf16_f32 v246, v246, v250\n\t"
    "v_cvt_pk_bf16_f32 v247, v247, v251\n\t"
    "ds_write_b128 v2, v[244:247] offset:3072\n\t"
    "ds_read_b128 v[232:235], v10 offset:128\n\t"
    "ds_read_b128 v[236:239], v10 offset:192\n\t"
    "s_waitcnt vmcnt(17)\n\t"
    "v_mfma_f32_16x16x32_bf16 v[112:115], a[40:43], a[0:3], 0\n\t"
    "s_waitcnt lgkmcnt(1)\n\t"
    "v_pk_add_f32 v[240:241], v[48:49], v[232:233]\n\t"
    "v_pk_add_f32 v[242:243], v[50:51], v[234:235]\n\t"
    "s_waitcnt lgkmcnt(0)\n\t"
    "v_pk_add_f32 v[248:249], v[64:65], v[236:237]\n\t"
    "v_mfma_f32_16x16x32_bf16 v[116:119], a[40:43], a[4:7], 0\n\t"
    "v_pk_add_f32 v[250:251], v[66:67], v[238:239]\n\t"
    "v_pk_mul_f32 v[240:241], v[240:241], v[160:161]\n\t"
    "v_pk_mul_f32 v[242:243], v[242:243], v[162:163]\n\t"
    "v_mfma_f32_16x16x32_bf16 v[120:123], a[40:43], a[8:11], 0\n\t"
    "v_pk_mul_f32 v[248:249], v[248:249], v[176:177]\n\t"
    "v_pk_mul_f32 v[250:251], v[250:251], v[178:179]\n\t"
    "v_cvt_pk_bf16_f32 v240, v240, v248\n\t"
    "v_mfma_f32_16x16x32_bf16 v[124:127], a[40:43], a[12:15], 0\n\t"
    "v_cvt_pk_bf16_f32 v241, v241, v249\n\t"
    "v_cvt_pk_bf16_f32 v242, v242, v250\n\t"
    "v_cvt_pk_bf16_f32 v243, v243, v251\n\t"
    "s_waitcnt vmcnt(16)\n\t"
    "v_mfma_f32_16x16x32_bf16 v[112:115], a[44:47], a[16:19], v[112:115]\n\t"
    "ds_write_b128 v2, v[240:243] offset:4096\n\t"
    "v_pk_add_f32 v[244:245], v[52:53], v[232:233]\n\t"
    "v_pk_add_f32 v[246:247], v[54:55], v[234:235]\n\t"
    "v_mfma_f32_16x16x32_bf16 v[116:119], a[44:47], a[20:23], v[116:119]\n\t"
    "v_pk_add_f32 v[248:249], v[68:69], v[236:237]\n\t"
    "v_pk_add_f32 v[250:251], v[70:71], v[238:239]\n\t"
    "v_pk_mul_f32 v[244:245], v[244:245], v[164:165]\n\t"
    "v_mfma_f32_16x16x32_bf16 v[120:123], a[44:47], a[24:27], v[120:123]\n\t"
    "v_pk_mul_f32 v[246:247], v[246:247], v[166:167]\n\t"
    "v_pk_mul_f32 v[248:249], v[248:249], v[180:181]\n\t"
    "v_pk_mul_f32 v[250:251], v[250:251], v[182:183]\n\t"
    "v_mfma_f32_16x16x32_bf16 v[124:127], a[44:47], a[28:31], v[124:127]\n\t"
    "v_cvt_pk_bf16_f32 v244, v244, v248\n\t"
    "v_cvt_pk_bf16_f32 v245, v245, v249\n\t"
    "v_cvt_pk_bf16_f32 v246, v246, v250\n\t"
    "s_waitcnt vmcnt(15)\n\t"
    "v_mfma_f32_16x16x32_bf16 v[112:115], a[80:83], a[48:51], v[112:115]\n\t"
    "v_cvt_pk_bf16_f32 v247, v247, v251\n\t"
    "ds_write_b128 v2, v[244:247] offset:5120\n\t"
    "v_pk_add_f32 v[240:241], v[56:57], v[232:233]\n\t"
    "v_mfma_f32_16x16x32_bf16 v[116:119], a[80:83], a[52:55], v[116:119]\n\t"
    "v_pk_add_f32 v[242:243], v[58:59], v[234:235]\n\t"
    "v_pk_add_f32 v[248:249], v[72:73], v[236:237]\n\t"
    "v_pk_add_f32 v[250:251], v[74:75], v[238:239]\n\t"
    "v_mfma_f32_16x16x32_bf16 v[120:123], a[80:83], a[56:59], v[120:123]\n\t"
    "v_pk_mul_f32 v[240:241], v[240:241], v[168:169]\n\t"
    "v_pk_mul_f32 v[242:243], v[242:243], v[170:171]\n\t"
    "v_pk_mul_f32 v[248:249], v[248:249], v[184:185]\n\t"
    "v_mfma_f32_16x16x32_bf16 v[124:127], a[80:83], a[60:63], v[124:127]\n\t"
    "v_pk_mul_f32 v[250:251], v[250:251], v[186:187]\n\t"
    "v_cvt_pk_bf16_f32 v240, v240, v248\n\t"
    "v_cvt_pk_bf16_f32 v241, v241, v249\n\t"
    "s_waitcnt vmcnt(14)\n\t"
    "v_mfma_f32_16x16x32_bf16 v[112:115], a[84:87], a[64:67], v[112:115]\n\t"
    "v_cvt_pk_bf16_f32 v242, v242, v250\n\t"
    "v_cvt_pk_bf16_f32 v243, v243, v251\n\t"
    "ds_write_b128 v2, v[240:243] offset:6144\n\t"
    "v_mfma_f32_16x16x32_bf16 v[116:119], a[84:87], a[68:71], v[116:119]\n\t"
    "v_pk_add_f32 v[244:245], v[60:61], v[232:233]\n\t"
    "v_pk_add_f32 v[246:247], v[62:63], v[234:235]\n\t"
    "v_pk_add_f32 v[248:249], v[76:77], v[236:237]\n\t"
    "v_mfma_f32_16x16x32_bf16 v[120:123], a[84:87], a[72:75], v[120:123]\n\t"
    "v_pk_add_f32 v[250:251], v[78:79], v[238:239]\n\t"
    "v_pk_mul_f32 v[244:245], v[244:245], v[172:173]\n\t"
    "v_pk_mul_f32 v[246:247], v[246:247], v[174:175]\n\t"
    "v_mfma_f32_16x16x32_bf16 v[124:127], a[84:87], a[76:79], v[124:127]\n\t"
    "v_pk_mul_f32 v[248:249], v[248:249], v[188:189]\n\t"
    "v_pk_mul_f32 v[250:251], v[250:251], v[190:191]\n\t"
    "v_cvt_pk_bf16_f32 v244, v244, v248\n\t"
    "s_waitcnt vmcnt(13)\n\t"
    "v_mfma_f32_16x16x32_bf16 v[112:115], a[88:91], a[112:115], v[112:115]\n\t"
    "v_cvt_pk_bf16_f32 v245, v245, v249\n\t"
    "v_cvt_pk_bf16_f32 v246, v246, v250\n\t"
    "v_cvt_pk_bf16_f32 v247, v247, v251\n\t"
    "v_mfma_f32_16x16x32_bf16 v[116:119], a[88:91], a[116:119], v[116:119]\n\t"
    "ds_write_b128 v2, v[244:247] offset:7168\n\t"
    "v_mfma_f32_16x16x32_bf16 v[120:123], a[88:91], a[120:123], v[120:123]\n\t"
    "v_mfma_f32_16x16x32_bf16 v[124:127], a[88:91], a[124:127], v[124:127]\n\t"
    "s_waitcnt vmcnt(12)\n\t"
    "v_mfma_f32_16x16x32_bf16 v[112:115], a[92:95], a[160:163], v[112:115]\n\t"
    "v_mfma_f32_16x16x32_bf16 v[116:119], a[92:95], a[164:167], v[116:119]\n\t"
    "v_mfma_f32_16x16x32_bf16 v[120:123], a[92:95], a[168:171], v[120:123]\n\t"
    "v_mfma_f32_16x16x32_bf16 v[124:127], a[92:95], a[172:175], v[124:127]\n\t"
    "s_waitcnt vmcnt(11)\n\t"
    "v_mfma_f32_16x16x32_bf16 v[112:115], a[128:131], a[192:195], v[112:115]\n\t"
    "v_mfma_f32_16x16x32_bf16 v[116:119], a[128:131], a[196:199], v[116:119]\n\t"
    "v_mfma_f32_16x16x32_bf16 v[120:123], a[128:131], a[200:203], v[120:123]\n\t"
    "v_mfma_f32_16x16x32_bf16 v[124:127], a[128:131], a[204:207], v[124:127]\n\t"
    "v_accvgpr_read_b32 v228, a248\n\t"
    "v_accvgpr_read_b32 v229, a249\n\t"
    "v_accvgpr_read_b32 v230, a250\n\t"
    "v_accvgpr_read_b32 v231, a251\n\t"
    "v_lshlrev_b32_e32 v224, 16, v228\n\t"
    "v_lshlrev_b32_e32 v225, 16, v229\n\t"
    "v_lshlrev_b32_e32 v226, 16, v230\n\t"
    "v_lshlrev_b32_e32 v227, 16, v231\n\t"
    "v_and_b32_e32 v228, 0xffff0000, v228\n\t"
    "v_and_b32_e32 v229, 0xffff0000, v229\n\t"
    "v_and_b32_e32 v230, 0xffff0000, v230\n\t"
    "v_and_b32_e32 v231, 0xffff0000, v231\n\t"
    "ds_write_b128 v7, v[112:115] offset:0\n\t"
    "ds_write_b128 v7, v[116:119] offset:1024\n\t"
    "ds_write_b128 v7, v[120:123] offset:2048\n\t"
    "ds_write_b128 v7, v[124:127] offset:3072\n\t"
    "ds_read_b128 v[232:235], v10 offset:384\n\t"
    "ds_read_b128 v[236:239], v10 offset:320\n\t"
    "s_waitcnt lgkmcnt(0)\n\t"
    "s_barrier\n\t"
    "ds_read_b128 v[248:251], v8 offset:0\n\t"
    "ds_read_b128 v[12:15], v8 offset:4096\n\t"
    "ds_read_b128 a[0:3], v2 offset:0\n\t"
    "ds_read_b128 a[4:7], v2 offset:1024\n\t"
    "ds_read_b128 a[8:11], v2 offset:2048\n\t"
    "ds_read_b128 a[12:15], v2 offset:3072\n\t"
    "ds_read_b128 a[16:19], v2 offset:4096\n\t"
    "ds_read_b128 a[20:23], v2 offset:5120\n\t"
    "ds_read_b128 a[24:27], v2 offset:6144\n\t"
    "ds_read_b128 a[28:31], v2 offset:7168\n\t"
    "s_waitcnt lgkmcnt(8)\n\t"
    "v_pk_add_f32 v[240:241], v[248:249], v[12:13]\n\t"
    "v_pk_add_f32 v[242:243], v[250:251], v[14:15]\n\t"
    "v_pk_add_f32 v[240:241], v[240:241], v[232:233]\n\t"
    "v_pk_add_f32 v[242:243], v[242:243], v[234:235]\n\t"
    "v_pk_mul_f32 v[240:241], v[240:241], v[224:225]\n\t"
    "v_pk_mul_f32 v[242:243], v[242:243], v[226:227]\n\t"
    "ds_read_b128 v[232:235], v10 offset:448\n\t"
    "ds_read_b128 v[248:251], v8 offset:8192\n\t"
    "ds_read_b128 v[12:15], v8 offset:12288\n\t"
    "ds_read_b128 a[48:51], v3 offset:0\n\t"
    "ds_read_b128 a[52:55], v3 offset:1024\n\t"
    "ds_read_b128 a[56:59], v3 offset:2048\n\t"
    "ds_read_b128 a[60:63], v3 offset:3072\n\t"
    "ds_read_b128 a[64:67], v3 offset:4096\n\t"
    "ds_read_b128 a[68:71], v3 offset:5120\n\t"
    "ds_read_b128 a[72:75], v3 offset:6144\n\t"
    "ds_read_b128 a[76:79], v3 offset:7168\n\t"
    "s_waitcnt lgkmcnt(8)\n\t"
    "v_pk_add_f32 v[248:249], v[248:249], v[12:13]\n\t"
    "v_pk_add_f32 v[250:251], v[250:251], v[14:15]\n\t"
    "v_pk_add_f32 v[248:249], v[248:249], v[232:233]\n\t"
    "v_pk_add_f32 v[250:251], v[250:251], v[234:235]\n\t"
    "v_pk_mul_f32 v[248:249], v[248:249], v[228:229]\n\t"
    "v_pk_mul_f32 v[250:251], v[250:251], v[230:231]\n\t"
    "v_cvt_pk_bf16_f32 v240, v240, v248\n\t"
    "v_cvt_pk_bf16_f32 v241, v241, v249\n\t"
    "v_cvt_pk_bf16_f32 v242, v242, v250\n\t"
    "v_cvt_pk_bf16_f32 v243, v243, v251\n\t"
    "ds_write_b128 v9, v[240:243] offset:0\n\t"
    "ds_read_b128 v[232:235], v10 offset:256\n\t"
    "ds_read_b128 a[96:99], v4 offset:0\n\t"
    "ds_read_b128 a[100:103], v4 offset:1024\n\t"
    "ds_read_b128 a[104:107], v4 offset:2048\n\t"
    "ds_read_b128 a[108:111], v4 offset:3072\n\t"
    "ds_read_b128 a[112:115], v4 offset:4096\n\t"
    "ds_read_b128 a[116:119], v4 offset:5120\n\t"
    "ds_read_b128 a[120:123], v4 offset:6144\n\t"
    "ds_read_b128 a[124:127], v4 offset:7168\n\t"
    "ds_read_b128 a[144:147], v5 offset:0\n\t"
    "ds_read_b128 a[148:151], v5 offset:1024\n\t"
    "ds_read_b128 a[152:155], v5 offset:2048\n\t"
    "ds_read_b128 a[156:159], v5 offset:3072\n\t"
    "ds_read_b128 a[160:163], v5 offset:4096\n\t"
    "ds_read_b128 a[164:167], v5 offset:5120\n\t"
    "ds_read_b128 a[168:171], v5 offset:6144\n\t"
    "ds_read_b128 a[172:175], v5 offset:7168\n\t"
    "v_add_u32_e32 v10, 2048, v10\n\t"
    "1:\n\t"
    "s_waitcnt vmcnt(8)\n\t"
    "v_mfma_f32_16x16x32_bf16 v[16:19], a[208:211], a[0:3], v[16:19]\n\t"
    "buffer_load_dword a248, v11, %[sW], s40 offen sc1\n\t"
    "buffer_load_dwordx4 a[248:251], v1, %[sW], s40 offen offset:0 sc1\n\t"
    "v_mfma_f32_16x16x32_bf16 v[32:35], a[212:215], a[0:3], v[32:35]\n\t"
    "s_waitcnt lgkmcnt(15)\n\t"
    "v_add_f32_e32 v240, v80, v232\n\t"
    "v_add_f32_e32 v241, v81, v233\n\t"
    "v_mfma_f32_16x16x32_bf16 v[36:39], a[212:215], a[4:7], v[36:39]\n\t"
    "v_add_f32_e32 v242, v82, v234\n\t"
    "v_add_f32_e32 v243, v83, v235\n\t"
    "v_mfma_f32_16x16x32_bf16 v[20:23], a[208:211], a[4:7], v[20:23]\n\t"
    "v_add_f32_e32 v248, v96, v236\n\t"
    "v_add_f32_e32 v249, v97, v237\n\t"
    "v_mfma_f32_16x16x32_bf16 v[24:27], a[208:211], a[8:11], v[24:27]\n\t"
    "buffer_load_dwordx4 a[252:255], v1, %[sW], s40 offen offset:1024 sc1\n\t"
    "v_add_f32_e32 v250, v98, v238\n\t"
    "v_mfma_f32_16x16x32_bf16 v[40:43], a[212:215], a[8:11], v[40:43]\n\t"
    "v_add_f32_e32 v251, v99, v239\n\t"
    "v_mul_f32_e32 v240, v240, v192\n\t"
    "v_mfma_f32_16x16x32_bf16 v[44:47], a[212:215], a[12:15], v[44:47]\n\t"
    "buffer_load_dwordx4 v[252:255], v1, %[sW], s40 offen offset:2048 sc1\n\t"
    "v_mul_f32_e32 v241, v241, v193\n\t"
    "v_mfma_f32_16x16x32_bf16 v[28:31], a[208:211], a[12:15], v[28:31]\n\t"
    "v_mul_f32_e32 v242, v242, v194\n\t"
    "v_mul_f32_e32 v243, v243, v195\n\t"
    "s_waitcnt vmcnt(10)\n\t"
    "v_mfma_f32_16x16x32_bf16 v[16:19], a[216:219], a[16:19], v[16:19]\n\t"
    "buffer_load_dwordx4 a[208:211], v1, %[sW], s40 offen offset:3072 sc1\n\t"
    "s_add_u32 s40, s40, 4096\n\t"
    "v_mfma_f32_16x16x32_bf16 v[32:35], a[220:223], a[16:19], v[32:35]\n\t"
    "v_mul_f32_e32 v248, v248, v208\n\t"
    "v_mul_f32_e32 v249, v249, v209\n\t"
    "v_mfma_f32_16x16x32_bf16 v[36:39], a[220:223], a[20:23], v[36:39]\n\t"
    "v_mul_f32_e32 v250, v250, v210\n\t"
    "v_mul_f32_e32 v251, v251, v211\n\t"
    "v_mfma_f32_16x16x32_bf16 v[20:23], a[216:219], a[20:23], v[20:23]\n\t"
    "v_cvt_pk_bf16_f32 v240, v240, v248\n\t"
    "v_cvt_pk_bf16_f32 v241, v241, v249\n\t"
    "v_mfma_f32_16x16x32_bf16 v[24:27], a[216:219], a[24:27], v[24:27]\n\t"
    "v_cvt_pk_bf16_f32 v242, v242, v250\n\t"
    "v_cvt_pk_bf16_f32 v243, v243, v251\n\t"
    "v_mfma_f32_16x16x32_bf16 v[40:43], a[220:223], a[24:27], v[40:43]\n\t"
    "ds_write_b128 v2, v[240:243] offset:8192\n\t"
    "v_add_f32_e32 v244, v84, v232\n\t"
    "v_mfma_f32_16x16x32_bf16 v[44:47], a[220:223], a[28:31], v[44:47]\n\t"
    "v_add_f32_e32 v245, v85, v233\n\t"
    "v_add_f32_e32 v246, v86, v234\n\t"
    "v_mfma_f32_16x16x32_bf16 v[28:31], a[216:219], a[28:31], v[28:31]\n\t"
    "v_add_f32_e32 v247, v87, v235\n\t"
    "v_add_f32_e32 v248, v100, v236\n\t"
    "s_waitcnt vmcnt(9)\n\t"
    "v_mfma_f32_16x16x32_bf16 v[16:19], a[224:227], a[48:51], v[16:19]\n\t"
    "buffer_load_dwordx4 a[216:219], v1, %[sW], s40 offen offset:0 sc1\n\t"
    "v_add_f32_e32 v249, v101, v237\n\t"
    "v_mfma_f32_16x16x32_bf16 v[32:35], a[228:231], a[48:51], v[32:35]\n\t"
    "v_add_f32_e32 v250, v102, v238\n\t"
    "v_add_f32_e32 v251, v103, v239\n\t"
    "v_mfma_f32_16x16x32_bf16 v[36:39], a[228:231], a[52:55], v[36:39]\n\t"
    "v_mul_f32_e32 v244, v244, v196\n\t"
    "v_mul_f32_e32 v245, v245, v197\n\t"
    "v_mfma_f32_16x16x32_bf16 v[20:23], a[224:227], a[52:55], v[20:23]\n\t"
    "v_mul_f32_e32 v246, v246, v198\n\t"
    "v_mul_f32_e32 v247, v247, v199\n\t"
    "v_mfma_f32_16x16x32_bf16 v[24:27], a[224:227], a[56:59], v[24:27]\n\t"
    "buffer_load_dwordx4 a[220:223], v1, %[sW], s40 offen offset:1024 sc1\n\t"
    "v_mul_f32_e32 v248, v248, v212\n\t"
    "v_mfma_f32_16x16x32_bf16 v[40:43], a[228:231], a[56:59], v[40:43]\n\t"
    "v_mul_f32_e32 v249, v249, v213\n\t"
    "v_mul_f32_e32 v250, v250, v214\n\t"
    "v_mfma_f32_16x16x32_bf16 v[44:47], a[228:231], a[60:63], v[44:47]\n\t"
    "v_mul_f32_e32 v251, v251, v215\n\t"
    "v_cvt_pk_bf16_f32 v244, v244, v248\n\t"
    "v_mfma_f32_16x16x32_bf16 v[28:31], a[224:227], a[60:63], v[28:31]\n\t"
    "v_cvt_pk_bf16_f32 v245, v245, v249\n\t"
    "v_cvt_pk_bf16_f32 v246, v246, v250\n\t"
    "s_waitcnt vmcnt(9)\n\t"
    "v_mfma_f32_16x16x32_bf16 v[16:19], a[232:235], a[64:67], v[16:19]\n\t"
    "buffer_load_dwordx4 a[224:227], v1, %[sW], s40 offen offset:2048 sc1\n\t"
    "v_cvt_pk_bf16_f32 v247, v247, v251\n\t"
    "v_mfma_f32_16x16x32_bf16 v[32:35], a[236:239], a[64:67], v[32:35]\n\t"
    "ds_write_b128 v2, v[244:247] offset:9216\n\t"
    "v_add_f32_e32 v240, v88, v232\n\t"
    "v_mfma_f32_16x16x32_bf16 v[36:39], a[236:239], a[68:71], v[36:39]\n\t"
    "v_add_f32_e32 v241, v89, v233\n\t"
    "v_add_f32_e32 v242, v90, v234\n\t"
    "v_mfma_f32_16x16x32_bf16 v[20:23], a[232:235], a[68:71], v[20:23]\n\t"
    "v_add_f32_e32 v243, v91, v235\n\t"
    "v_add_f32_e32 v248, v104, v236\n\t"
    "v_mfma_f32_16x16x32_bf16 v[24:27], a[232:235], a[72:75], v[24:27]\n\t"
    "buffer_load_dwordx4 a[228:231], v1, %[sW], s40 offen offset:3072 sc1\n\t"
    "s_add_u32 s40, s40, 4096\n\t"
    "v_mfma_f32_16x16x32_bf16 v[40:43], a[236:239], a[72:75], v[40:43]\n\t"
    "v_add_f32_e32 v249, v105, v237\n\t"
    "v_add_f32_e32 v250, v106, v238\n\t"
    "v_mfma_f32_16x16x32_bf16 v[44:47], a[236:239], a[76:79], v[44:47]\n\t"
    "v_add_f32_e32 v251, v107, v239\n\t"
    "v_mul_f32_e32 v240, v240, v200\n\t"
    "v_mfma_f32_16x16x32_bf16 v[28:31], a[232:235], a[76:79], v[28:31]\n\t"
    "v_mul_f32_e32 v241, v241, v201\n\t"
    "v_mul_f32_e32 v242, v242, v202\n\t"
    "s_waitcnt vmcnt(9)\n\t"
    "v_mfma_f32_16x16x32_bf16 v[16:19], a[240:243], a[96:99], v[16:19]\n\t"
    "buffer_load_dwordx4 a[232:235], v1, %[sW], s40 offen offset:0 sc1\n\t"
    "v_mul_f32_e32 v243, v243, v203\n\t"
    "v_mfma_f32_16x16x32_bf16 v[32:35], a[244:247], a[96:99], v[32:35]\n\t"
    "v_mul_f32_e32 v248, v248, v216\n\t"
    "v_mul_f32_e32 v249, v249, v217\n\t"
    "s_waitcnt lgkmcnt(15)\n\t"
    "v_mfma_f32_16x16x32_bf16 v[36:39], a[244:247], a[100:103], v[36:39]\n\t"
    "v_mul_f32_e32 v250, v250, v218\n\t"
    "v_mul_f32_e32 v251, v251, v219\n\t"
    "v_mfma_f32_16x16x32_bf16 v[20:23], a[240:243], a[100:103], v[20:23]\n\t"
    "v_cvt_pk_bf16_f32 v240, v240, v248\n\t"
    "v_cvt_pk_bf16_f32 v241, v241, v249\n\t"
    "v_mfma_f32_16x16x32_bf16 v[24:27], a[240:243], a[104:107], v[24:27]\n\t"
    "buffer_load_dwordx4 a[236:239], v1, %[sW], s40 offen offset:1024 sc1\n\t"
    "v_cvt_pk_bf16_f32 v242, v242, v250\n\t"
    "v_mfma_f32_16x16x32_bf16 v[40:43], a[244:247], a[104:107], v[40:43]\n\t"
    "v_cvt_pk_bf16_f32 v243, v243, v251\n\t"
    "ds_write_b128 v2, v[240:243] offset:10240\n\t"
    "s_waitcnt lgkmcnt(15)\n\t"
    "v_mfma_f32_16x16x32_bf16 v[44:47], a[244:247], a[108:111], v[44:47]\n\t"
    "v_add_f32_e32 v244, v92, v232\n\t"
    "v_add_f32_e32 v245, v93, v233\n\t"
    "v_mfma_f32_16x16x32_bf16 v[28:31], a[240:243], a[108:111], v[28:31]\n\t"
    "v_add_f32_e32 v246, v94, v234\n\t"
    "v_add_f32_e32 v247, v95, v235\n\t"
    "s_waitcnt vmcnt(8) lgkmcnt(14)\n\t"
    "v_mfma_f32_16x16x32_bf16 v[16:19], a[248:251], a[112:115], v[16:19]\n\t"
    "buffer_load_dwordx4 a[240:243], v1, %[sW], s40 offen offset:2048 sc1\n\t"
    "v_add_f32_e32 v248, v108, v236\n\t"
    "v_mfma_f32_16x16x32_bf16 v[32:35], a[252:255], a[112:115], v[32:35]\n\t"
    "v_add_f32_e32 v249, v109, v237\n\t"
    "v_add_f32_e32 v250, v110, v238\n\t"
    "s_waitcnt lgkmcnt(13)\n\t"
    "v_mfma_f32_16x16x32_bf16 v[36:39], a[252:255], a[116:119], v[36:39]\n\t"
    "v_add_f32_e32 v251, v111, v239\n\t"
    "v_mul_f32_e32 v244, v244, v204\n\t"
    "v_mfma_f32_16x16x32_bf16 v[20:23], a[248:251], a[116:119], v[20:23]\n\t"
    "v_mul_f32_e32 v245, v245, v205\n\t"
    "v_mul_f32_e32 v246, v246, v206\n\t"
    "s_waitcnt lgkmcnt(12)\n\t"
    "v_mfma_f32_16x16x32_bf16 v[24:27], a[248:251], a[120:123], v[24:27]\n\t"
    "buffer_load_dwordx4 a[244:247], v1, %[sW], s40 offen offset:3072 sc1\n\t"
    "s_add_u32 s40, s40, 4096\n\t"
    "v_mfma_f32_16x16x32_bf16 v[40:43], a[252:255], a[120:123], v[40:43]\n\t"
    "v_mul_f32_e32 v247, v247, v207\n\t"
    "v_mul_f32_e32 v248, v248, v220\n\t"
    "s_waitcnt lgkmcnt(11)\n\t"
    "v_mfma_f32_16x16x32_bf16 v[44:47], a[252:255], a[124:127], v[44:47]\n\t"
    "v_mul_f32_e32 v249, v249, v221\n\t"
    "v_mul_f32_e32 v250, v250, v222\n\t"
    "v_mfma_f32_16x16x32_bf16 v[28:31], a[248:251], a[124:127], v[28:31]\n\t"
    "v_mul_f32_e32 v251, v251, v223\n\t"
    "v_cvt_pk_bf16_f32 v244, v244, v248\n\t"
    "s_waitcnt vmcnt(8) lgkmcnt(10)\n\t"
    "v_mfma_f32_16x16x32_bf16 v[16:19], v[252:255], a[144:147], v[16:19]\n\t"
    "buffer_load_dwordx4 a[248:251], v1, %[sW], s40 offen offset:0 sc1\n\t"
    "v_cvt_pk_bf16_f32 v245, v245, v249\n\t"
    "v_mfma_f32_16x16x32_bf16 v[32:35], a[208:211], a[144:147], v[32:35]\n\t"
    "v_cvt_pk_bf16_f32 v246, v246, v250\n\t"
    "v_cvt_pk_bf16_f32 v247, v247, v251\n\t"
    "s_waitcnt lgkmcnt(9)\n\t"
    "v_mfma_f32_16x16x32_bf16 v[36:39], a[208:211], a[148:151], v[36:39]\n\t"
    "ds_write_b128 v2, v[244:247] offset:11264\n\t"
    "ds_read_b128 v[232:235], v10 offset:0\n\t"
    "v_mfma_f32_16x16x32_bf16 v[20:23], v[252:255], a[148:151], v[20:23]\n\t"
    "ds_read_b128 v[236:239], v10 offset:64\n\t"
    "s_waitcnt lgkmcnt(0)\n\t"
    "v_mfma_f32_16x16x32_bf16 v[24:27], v[252:255], a[152:155], v[24:27]\n\t"
    "buffer_load_dwordx4 a[252:255], v1, %[sW], s40 offen offset:1024 sc1\n\t"
    "s_barrier\n\t"
    "v_mfma_f32_16x16x32_bf16 v[40:43], a[208:211], a[152:155], v[40:43]\n\t"
    "ds_read_b128 a[192:195], v6 offset:49152\n\t"
    "ds_read_b128 a[196:199], v6 offset:50176\n\t"
    "v_mfma_f32_16x16x32_bf16 v[44:47], a[208:211], a[156:159], v[44:47]\n\t"
    "ds_read_b128 a[200:203], v6 offset:51200\n\t"
    "ds_read_b128 a[204:207], v6 offset:52224\n\t"
    "v_mfma_f32_16x16x32_bf16 v[28:31], v[252:255], a[156:159], v[28:31]\n\t"
    "ds_read_b128 a[32:35], v2 offset:8192\n\t"
    "ds_read_b128 a[36:39], v2 offset:9216\n\t"
    "s_waitcnt vmcnt(8)\n\t"
    "v_mfma_f32_16x16x32_bf16 v[16:19], a[216:219], a[160:163], v[16:19]\n\t"
    "buffer_load_dwordx4 v[252:255], v1, %[sW], s40 offen offset:2048 sc1\n\t"
    "ds_read_b128 a[40:43], v2 offset:10240\n\t"
    "v_mfma_f32_16x16x32_bf16 v[32:35], a[220:223], a[160:163], v[32:35]\n\t"
    "ds_read_b128 a[44:47], v2 offset:11264\n\t"
    "ds_read_b128 a[80:83], v3 offset:8192\n\t"
    "v_mfma_f32_16x16x32_bf16 v[36:39], a[220:223], a[164:167], v[36:39]\n\t"
    "ds_read_b128 a[84:87], v3 offset:9216\n\t"
    "ds_read_b128 a[88:91], v3 offset:10240\n\t"
    "v_mfma_f32_16x16x32_bf16 v[20:23], a[216:219], a[164:167], v[20:23]\n\t"
    "ds_read_b128 a[92:95], v3 offset:11264\n\t"
    "ds_read_b128 a[128:131], v4 offset:8192\n\t"
    "v_mfma_f32_16x16x32_bf16 v[24:27], a[216:219], a[168:171], v[24:27]\n\t"
    "buffer_load_dwordx4 a[208:211], v1, %[sW], s40 offen offset:3072 sc1\n\t"
    "s_add_u32 s40, s40, 4096\n\t"
    "v_mfma_f32_16x16x32_bf16 v[40:43], a[220:223], a[168:171], v[40:43]\n\t"
    "ds_read_b128 a[132:135], v4 offset:9216\n\t"
    "ds_read_b128 a[136:139], v4 offset:10240\n\t"
    "v_mfma_f32_16x16x32_bf16 v[44:47], a[220:223], a[172:175], v[44:47]\n\t"
    "buffer_load_dwordx4 a[212:215], v1, %[sW], s40 offen offset:0 sc1\n\t"
    "ds_read_b128 a[140:143], v4 offset:11264\n\t"
    "v_mfma_f32_16x16x32_bf16 v[28:31], a[216:219], a[172:175], v[28:31]\n\t"
    "ds_read_b128 a[176:179], v5 offset:8192\n\t"
    "ds_read_b128 a[180:183], v5 offset:9216\n\t"
    "s_waitcnt vmcnt(10)\n\t"
    "v_mfma_f32_16x16x32_bf16 v[112:115], a[224:227], a[0:3], v[112:115]\n\t"
    "buffer_load_dwordx4 a[216:219], v1, %[sW], s40 offen offset:1024 sc1\n\t"
    "ds_read_b128 a[184:187], v5 offset:10240\n\t"
    "v_mfma_f32_16x16x32_bf16 v[116:119], a[224:227], a[4:7], v[116:119]\n\t"
    "ds_read_b128 a[188:191], v5 offset:11264\n\t"
    "v_mfma_f32_16x16x32_bf16 v[120:123], a[224:227], a[8:11], v[120:123]\n\t"
    "v_mfma_f32_16x16x32_bf16 v[124:127], a[224:227], a[12:15], v[124:127]\n\t"
    "s_waitcnt vmcnt(10)\n\t"
    "v_mfma_f32_16x16x32_bf16 v[112:115], a[228:231], a[16:19], v[112:115]\n\t"
    "buffer_load_dwordx4 a[224:227], v1, %[sW], s40 offen offset:2048 sc1\n\t"
    "v_mfma_f32_16x16x32_bf16 v[116:119], a[228:231], a[20:23], v[116:119]\n\t"
    "v_mfma_f32_16x16x32_bf16 v[120:123], a[228:231], a[24:27], v[120:123]\n\t"
    "v_mfma_f32_16x16x32_bf16 v[124:127], a[228:231], a[28:31], v[124:127]\n\t"
    "s_waitcnt vmcnt(10)\n\t"
    "v_mfma_f32_16x16x32_bf16 v[112:115], a[232:235], a[96:99], v[112:115]\n\t"
    "buffer_load_dwordx4 a[228:231], v1, %[sW], s40 offen offset:3072 sc1\n\t"
    "s_add_u32 s40, s40, 4096\n\t"
    "v_mfma_f32_16x16x32_bf16 v[116:119], a[232:235], a[100:103], v[116:119]\n\t"
    "v_mfma_f32_16x16x32_bf16 v[120:123], a[232:235], a[104:107], v[120:123]\n\t"
    "v_mfma_f32_16x16x32_bf16 v[124:127], a[232:235], a[108:111], v[124:127]\n\t"
    "s_waitcnt vmcnt(10)\n\t"
    "v_mfma_f32_16x16x32_bf16 v[112:115], a[236:239], a[112:115], v[112:115]\n\t"
    "buffer_load_dwordx4 a[232:235], v1, %[sW], s40 offen offset:0 sc1\n\t"
    "v_mfma_f32_16x16x32_bf16 v[116:119], a[236:239], a[116:119], v[116:119]\n\t"
    "v_mfma_f32_16x16x32_bf16 v[120:123], a[236:239], a[120:123], v[120:123]\n\t"
    "v_mfma_f32_16x16x32_bf16 v[124:127], a[236:239], a[124:127], v[124:127]\n\t"
    "s_waitcnt vmcnt(9) lgkmcnt(15)\n\t"
    "v_mfma_f32_16x16x32_bf16 v[16:19], a[240:243], a[192:195], v[16:19]\n\t"
    "buffer_load_dwordx4 a[236:239], v1, %[sW], s40 offen offset:1024 sc1\n\t"
    "v_mfma_f32_16x16x32_bf16 v[32:35], a[244:247], a[192:195], v[32:35]\n\t"
    "v_mfma_f32_16x16x32_bf16 v[36:39], a[244:247], a[196:199], v[36:39]\n\t"
    "v_mfma_f32_16x16x32_bf16 v[20:23], a[240:243], a[196:199], v[20:23]\n\t"
    "v_mfma_f32_16x16x32_bf16 v[24:27], a[240:243], a[200:203], v[24:27]\n\t"
    "v_mfma_f32_16x16x32_bf16 v[40:43], a[244:247], a[200:203], v[40:43]\n\t"
    "v_mfma_f32_16x16x32_bf16 v[44:47], a[244:247], a[204:207], v[44:47]\n\t"
    "v_mfma_f32_16x16x32_bf16 v[28:31], a[240:243], a[204:207], v[28:31]\n\t"
    "s_waitcnt vmcnt(8)\n\t"
    "v_mfma_f32_16x16x32_bf16 v[16:19], a[248:251], a[32:35], v[16:19]\n\t"
    "buffer_load_dwordx4 a[240:243], v1, %[sW], s40 offen offset:2048 sc1\n\t"
    "v_mfma_f32_16x16x32_bf16 v[32:35], a[252:255], a[32:35], v[32:35]\n\t"
    "s_waitcnt lgkmcnt(14)\n\t"
    "v_mfma_f32_16x16x32_bf16 v[36:39], a[252:255], a[36:39], v[36:39]\n\t"
    "v_mfma_f32_16x16x32_bf16 v[20:23], a[248:251], a[36:39], v[20:23]\n\t"
    "s_waitcnt lgkmcnt(13)\n\t"
    "v_mfma_f32_16x16x32_bf16 v[24:27], a[248:251], a[40:43], v[24:27]\n\t"
    "buffer_load_dwordx4 a[244:247], v1, %[sW], s40 offen offset:3072 sc1\n\t"
    "s_add_u32 s40, s40, 4096\n\t"
    "v_mfma_f32_16x16x32_bf16 v[40:43], a[252:255], a[40:43], v[40:43]\n\t"
    "s_waitcnt lgkmcnt(12)\n\t"
    "v_mfma_f32_16x16x32_bf16 v[44:47], a[252:255], a[44:47], v[44:47]\n\t"
    "v_mfma_f32_16x16x32_bf16 v[28:31], a[248:251], a[44:47], v[28:31]\n\t"
    "s_waitcnt vmcnt(8) lgkmcnt(11)\n\t"
    "v_mfma_f32_16x16x32_bf16 v[16:19], v[252:255], a[80:83], v[16:19]\n\t"
    "buffer_load_dwordx4 a[248:251], v1, %[sW], s40 offen offset:0 sc1\n\t"
    "v_mfma_f32_16x16x32_bf16 v[32:35], a[208:211], a[80:83], v[32:35]\n\t"
    "s_waitcnt lgkmcnt(10)\n\t"
    "v_mfma_f32_16x16x32_bf16 v[36:39], a[208:211], a[84:87], v[36:39]\n\t"
    "v_mfma_f32_16x16x32_bf16 v[20:23], v[252:255], a[84:87], v[20:23]\n\t"
    "s_waitcnt lgkmcnt(9)\n\t"
    "v_mfma_f32_16x16x32_bf16 v[24:27], v[252:255], a[88:91], v[24:27]\n\t"
    "buffer_load_dwordx4 a[252:255], v1, %[sW], s40 offen offset:1024 sc1\n\t"
    "v_mfma_f32_16x16x32_bf16 v[40:43], a[208:211], a[88:91], v[40:43]\n\t"
    "s_waitcnt lgkmcnt(8)\n\t"
    "v_mfma_f32_16x16x32_bf16 v[44:47], a[208:211], a[92:95], v[44:47]\n\t"
    "v_mfma_f32_16x16x32_bf16 v[28:31], v[252:255], a[92:95], v[28:31]\n\t"
    "s_waitcnt vmcnt(8) lgkmcnt(7)\n\t"
    "v_mfma_f32_16x16x32_bf16 v[16:19], a[212:215], a[128:131], v[16:19]\n\t"
    "buffer_load_dwordx4 v[252:255], v1, %[sW], s40 offen offset:2048 sc1\n\t"
    "v_mfma_f32_16x16x32_bf16 v[32:35], a[216:219], a[128:131], v[32:35]\n\t"
    "s_waitcnt lgkmcnt(6)\n\t"
    "v_mfma_f32_16x16x32_bf16 v[36:39], a[216:219], a[132:135], v[36:39]\n\t"
    "v_mfma_f32_16x16x32_bf16 v[20:23], a[212:215], a[132:135], v[20:23]\n\t"
    "s_waitcnt lgkmcnt(5)\n\t"
    "v_mfma_f32_16x16x32_bf16 v[24:27], a[212:215], a[136:139], v[24:27]\n\t"
    "buffer_load_dwordx4 a[208:211], v1, %[sW], s40 offen offset:3072 sc1\n\t"
    "s_add_u32 s40, s40, 4096\n\t"
    "v_mfma_f32_16x16x32_bf16 v[40:43], a[216:219], a[136:139], v[40:43]\n\t"
    "s_waitcnt lgkmcnt(4)\n\t"
    "v_mfma_f32_16x16x32_bf16 v[44:47], a[216:219], a[140:143], v[44:47]\n\t"
    "v_mfma_f32_16x16x32_bf16 v[28:31], a[212:215], a[140:143], v[28:31]\n\t"
    "s_waitcnt vmcnt(8) lgkmcnt(3)\n\t"
    "v_mfma_f32_16x16x32_bf16 v[16:19], a[224:227], a[176:179], v[16:19]\n\t"
    "buffer_load_dword a212, v11, %[sW], s40 offen sc1\n\t"
    "buffer_load_dwordx4 a[212:215], v1, %[sW], s40 offen offset:0 sc1\n\t"
    "v_mfma_f32_16x16x32_bf16 v[32:35], a[228:231], a[176:179], v[32:35]\n\t"
    "s_waitcnt lgkmcnt(2)\n\t"
    "v_mfma_f32_16x16x32_bf16 v[36:39], a[228:231], a[180:183], v[36:39]\n\t"
    "v_mfma_f32_16x16x32_bf16 v[20:23], a[224:227], a[180:183], v[20:23]\n\t"
    "s_waitcnt lgkmcnt(1)\n\t"
    "v_mfma_f32_16x16x32_bf16 v[24:27], a[224:227], a[184:187], v[24:27]\n\t"
    "buffer_load_dwordx4 a[216:219], v1, %[sW], s40 offen offset:1024 sc1\n\t"
    "v_mfma_f32_16x16x32_bf16 v[40:43], a[228:231], a[184:187], v[40:43]\n\t"
    "s_waitcnt lgkmcnt(0)\n\t"
    "v_mfma_f32_16x16x32_bf16 v[44:47], a[228:231], a[188:191], v[44:47]\n\t"
    "buffer_load_dwordx4 a[220:223], v1, %[sW], s40 offen offset:2048 sc1\n\t"
    "v_mfma_f32_16x16x32_bf16 v[28:31], a[224:227], a[188:191], v[28:31]\n\t"
    "s_waitcnt vmcnt(11)\n\t"
    "v_mfma_f32_16x16x32_bf16 v[112:115], a[232:235], a[192:195], v[112:115]\n\t"
    "buffer_load_dwordx4 a[224:227], v1, %[sW], s40 offen offset:3072 sc1\n\t"
    "s_add_u32 s40, s40, 4096\n\t"
    "v_mfma_f32_16x16x32_bf16 v[116:119], a[232:235], a[196:199], v[116:119]\n\t"
    "v_mfma_f32_16x16x32_bf16 v[120:123], a[232:235], a[200:203], v[120:123]\n\t"
    "v_add_f32_e32 v240, v16, v232\n\t"
    "v_add_f32_e32 v241, v17, v233\n\t"
    "v_mfma_f32_16x16x32_bf16 v[124:127], a[232:235], a[204:207], v[124:127]\n\t"
    "v_add_f32_e32 v242, v18, v234\n\t"
    "v_add_f32_e32 v243, v19, v235\n\t"
    "s_waitcnt vmcnt(11)\n\t"
    "v_mfma_f32_16x16x32_bf16 v[112:115], a[236:239], a[32:35], v[112:115]\n\t"
    "buffer_load_dwordx4 a[232:235], v1, %[sW], s40 offen offset:0 sc1\n\t"
    "v_add_f32_e32 v248, v32, v236\n\t"
    "v_mfma_f32_16x16x32_bf16 v[116:119], a[236:239], a[36:39], v[116:119]\n\t"
    "v_add_f32_e32 v249, v33, v237\n\t"
    "v_add_f32_e32 v250, v34, v238\n\t"
    "v_mfma_f32_16x16x32_bf16 v[120:123], a[236:239], a[40:43], v[120:123]\n\t"
    "v_add_f32_e32 v251, v35, v239\n\t"
    "v_mul_f32_e32 v240, v240, v128\n\t"
    "v_mfma_f32_16x16x32_bf16 v[124:127], a[236:239], a[44:47], v[124:127]\n\t"
    "v_mul_f32_e32 v241, v241, v129\n\t"
    "v_mul_f32_e32 v242, v242, v130\n\t"
    "s_waitcnt vmcnt(11)\n\t"
    "v_mfma_f32_16x16x32_bf16 v[112:115], a[240:243], a[128:131], v[112:115]\n\t"
    "buffer_load_dwordx4 a[236:239], v1, %[sW], s40 offen offset:1024 sc1\n\t"
    "v_mul_f32_e32 v243, v243, v131\n\t"
    "v_mfma_f32_16x16x32_bf16 v[116:119], a[240:243], a[132:135], v[116:119]\n\t"
    "v_mul_f32_e32 v248, v248, v144\n\t"
    "v_mul_f32_e32 v249, v249, v145\n\t"
    "v_mfma_f32_16x16x32_bf16 v[120:123], a[240:243], a[136:139], v[120:123]\n\t"
    "v_mul_f32_e32 v250, v250, v146\n\t"
    "v_mul_f32_e32 v251, v251, v147\n\t"
    "v_mfma_f32_16x16x32_bf16 v[124:127], a[240:243], a[140:143], v[124:127]\n\t"
    "v_cvt_pk_bf16_f32 v240, v240, v248\n\t"
    "v_cvt_pk_bf16_f32 v241, v241, v249\n\t"
    "s_waitcnt vmcnt(10)\n\t"
    "v_mfma_f32_16x16x32_bf16 v[48:51], a[244:247], a[0:3], v[48:51]\n\t"
    "buffer_load_dwordx4 a[240:243], v1, %[sW], s40 offen offset:2048 sc1\n\t"
    "v_cvt_pk_bf16_f32 v242, v242, v250\n\t"
    "v_mfma_f32_16x16x32_bf16 v[64:67], a[248:251], a[0:3], v[64:67]\n\t"
    "v_cvt_pk_bf16_f32 v243, v243, v251\n\t"
    "ds_write_b128 v2, v[240:243] offset:0\n\t"
    "v_mfma_f32_16x16x32_bf16 v[68:71], a[248:251], a[4:7], v[68:71]\n\t"
    "v_add_f32_e32 v244, v20, v232\n\t"
    "v_add_f32_e32 v245, v21, v233\n\t"
    "v_mfma_f32_16x16x32_bf16 v[52:55], a[244:247], a[4:7], v[52:55]\n\t"
    "v_add_f32_e32 v246, v22, v234\n\t"
    "v_add_f32_e32 v247, v23, v235\n\t"
    "v_mfma_f32_16x16x32_bf16 v[56:59], a[244:247], a[8:11], v[56:59]\n\t"
    "v_add_f32_e32 v248, v36, v236\n\t"
    "v_add_f32_e32 v249, v37, v237\n\t"
    "v_mfma_f32_16x16x32_bf16 v[72:75], a[248:251], a[8:11], v[72:75]\n\t"
    "v_add_f32_e32 v250, v38, v238\n\t"
    "v_add_f32_e32 v251, v39, v239\n\t"
    "v_mfma_f32_16x16x32_bf16 v[76:79], a[248:251], a[12:15], v[76:79]\n\t"
    "v_mul_f32_e32 v244, v244, v132\n\t"
    "v_mul_f32_e32 v245, v245, v133\n\t"
    "v_mfma_f32_16x16x32_bf16 v[60:63], a[244:247], a[12:15], v[60:63]\n\t"
    "v_mul_f32_e32 v246, v246, v134\n\t"
    "v_mul_f32_e32 v247, v247, v135\n\t"
    "s_waitcnt vmcnt(9)\n\t"
    "v_mfma_f32_16x16x32_bf16 v[48:51], a[252:255], a[16:19], v[48:51]\n\t"
    "buffer_load_dwordx4 a[244:247], v1, %[sW], s40 offen offset:3072 sc1\n\t"
    "s_add_u32 s40, s40, 4096\n\t"
    "v_mfma_f32_16x16x32_bf16 v[64:67], v[252:255], a[16:19], v[64:67]\n\t"
    "v_mul_f32_e32 v248, v248, v148\n\t"
    "v_mul_f32_e32 v249, v249, v149\n\t"
    "v_mfma_f32_16x16x32_bf16 v[68:71], v[252:255], a[20:23], v[68:71]\n\t"
    "v_mul_f32_e32 v250, v250, v150\n\t"
    "v_mul_f32_e32 v251, v251, v151\n\t"
    "v_mfma_f32_16x16x32_bf16 v[52:55], a[252:255], a[20:23], v[52:55]\n\t"
    "v_cvt_pk_bf16_f32 v244, v244, v248\n\t"
    "v_cvt_pk_bf16_f32 v245, v245, v249\n\t"
    "v_mfma_f32_16x16x32_bf16 v[56:59], a[252:255], a[24:27], v[56:59]\n\t"
    "buffer_load_dwordx4 a[248:251], v1, %[sW], s40 offen offset:0 sc1\n\t"
    "v_cvt_pk_bf16_f32 v246, v246, v250\n\t"
    "v_mfma_f32_16x16x32_bf16 v[72:75], v[252:255], a[24:27], v[72:75]\n\t"
    "v_cvt_pk_bf16_f32 v247, v247, v251\n\t"
    "ds_write_b128 v2, v[244:247] offset:1024\n\t"
    "v_mfma_f32_16x16x32_bf16 v[76:79], v[252:255], a[28:31], v[76:79]\n\t"
    "v_add_f32_e32 v240, v24, v232\n\t"
    "v_add_f32_e32 v241, v25, v233\n\t"
    "v_mfma_f32_16x16x32_bf16 v[60:63], a[252:255], a[28:31], v[60:63]\n\t"
    "v_add_f32_e32 v242, v26, v234\n\t"
    "v_add_f32_e32 v243, v27, v235\n\t"
    "s_waitcnt vmcnt(8)\n\t"
    "v_mfma_f32_16x16x32_bf16 v[48:51], a[208:211], a[48:51], v[48:51]\n\t"
    "buffer_load_dwordx4 a[252:255], v1, %[sW], s40 offen offset:1024 sc1\n\t"
    "v_add_f32_e32 v248, v40, v236\n\t"
    "v_mfma_f32_16x16x32_bf16 v[64:67], a[212:215], a[48:51], v[64:67]\n\t"
    "v_add_f32_e32 v249, v41, v237\n\t"
    "v_add_f32_e32 v250, v42, v238\n\t"
    "v_mfma_f32_16x16x32_bf16 v[68:71], a[212:215], a[52:55], v[68:71]\n\t"
    "v_add_f32_e32 v251, v43, v239\n\t"
    "v_mul_f32_e32 v240, v240, v136\n\t"
    "v_mfma_f32_16x16x32_bf16 v[52:55], a[208:211], a[52:55], v[52:55]\n\t"
    "v_mul_f32_e32 v241, v241, v137\n\t"
    "v_mul_f32_e32 v242, v242, v138\n\t"
    "v_mfma_f32_16x16x32_bf16 v[56:59], a[208:211], a[56:59], v[56:59]\n\t"
    "buffer_load_dwordx4 v[252:255], v1, %[sW], s40 offen offset:2048 sc1\n\t"
    "v_mul_f32_e32 v243, v243, v139\n\t"
    "v_mfma_f32_16x16x32_bf16 v[72:75], a[212:215], a[56:59], v[72:75]\n\t"
    "v_mul_f32_e32 v248, v248, v152\n\t"
    "v_mul_f32_e32 v249, v249, v153\n\t"
    "v_mfma_f32_16x16x32_bf16 v[76:79], a[212:215], a[60:63], v[76:79]\n\t"
    "v_mul_f32_e32 v250, v250, v154\n\t"
    "v_mul_f32_e32 v251, v251, v155\n\t"
    "v_mfma_f32_16x16x32_bf16 v[60:63], a[208:211], a[60:63], v[60:63]\n\t"
    "v_cvt_pk_bf16_f32 v240, v240, v248\n\t"
    "v_cvt_pk_bf16_f32 v241, v241, v249\n\t"
    "s_waitcnt vmcnt(8)\n\t"
    "v_mfma_f32_16x16x32_bf16 v[48:51], a[216:219], a[64:67], v[48:51]\n\t"
    "buffer_load_dwordx4 a[208:211], v1, %[sW], s40 offen offset:3072 sc1\n\t"
    "s_add_u32 s40, s40, 4096\n\t"
    "v_mfma_f32_16x16x32_bf16 v[64:67], a[220:223], a[64:67], v[64:67]\n\t"
    "v_cvt_pk_bf16_f32 v242, v242, v250\n\t"
    "v_cvt_pk_bf16_f32 v243, v243, v251\n\t"
    "v_mfma_f32_16x16x32_bf16 v[68:71], a[220:223], a[68:71], v[68:71]\n\t"
    "ds_write_b128 v2, v[240:243] offset:2048\n\t"
    "v_add_f32_e32 v244, v28, v232\n\t"
    "v_mfma_f32_16x16x32_bf16 v[52:55], a[216:219], a[68:71], v[52:55]\n\t"
    "v_add_f32_e32 v245, v29, v233\n\t"
    "v_add_f32_e32 v246, v30, v234\n\t"
    "v_mfma_f32_16x16x32_bf16 v[56:59], a[216:219], a[72:75], v[56:59]\n\t"
    "buffer_load_dwordx4 a[212:215], v1, %[sW], s40 offen offset:0 sc1\n\t"
    "v_add_f32_e32 v247, v31, v235\n\t"
    "v_mfma_f32_16x16x32_bf16 v[72:75], a[220:223], a[72:75], v[72:75]\n\t"
    "v_add_f32_e32 v248, v44, v236\n\t"
    "v_add_f32_e32 v249, v45, v237\n\t"
    "v_mfma_f32_16x16x32_bf16 v[76:79], a[220:223], a[76:79], v[76:79]\n\t"
    "v_add_f32_e32 v250, v46, v238\n\t"
    "v_add_f32_e32 v251, v47, v239\n\t"
    "v_mfma_f32_16x16x32_bf16 v[60:63], a[216:219], a[76:79], v[60:63]\n\t"
    "v_mul_f32_e32 v244, v244, v140\n\t"
    "v_mul_f32_e32 v245, v245, v141\n\t"
    "s_waitcnt vmcnt(8)\n\t"
    "v_mfma_f32_16x16x32_bf16 v[48:51], a[224:227], a[96:99], v[48:51]\n\t"
    "buffer_load_dwordx4 a[216:219], v1, %[sW], s40 offen offset:1024 sc1\n\t"
    "v_mul_f32_e32 v246, v246, v142\n\t"
    "v_mfma_f32_16x16x32_bf16 v[64:67], a[232:235], a[96:99], v[64:67]\n\t"
    "v_mul_f32_e32 v247, v247, v143\n\t"
    "v_mul_f32_e32 v248, v248, v156\n\t"
    "v_mfma_f32_16x16x32_bf16 v[68:71], a[232:235], a[100:103], v[68:71]\n\t"
    "v_mul_f32_e32 v249, v249, v157\n\t"
    "v_mul_f32_e32 v250, v250, v158\n\t"
    "v_mfma_f32_16x16x32_bf16 v[52:55], a[224:227], a[100:103], v[52:55]\n\t"
    "v_mul_f32_e32 v251, v251, v159\n\t"
    "v_cvt_pk_bf16_f32 v244, v244, v248\n\t"
    "v_mfma_f32_16x16x32_bf16 v[56:59], a[224:227], a[104:107], v[56:59]\n\t"
    "buffer_load_dwordx4 a[220:223], v1, %[sW], s40 offen offset:2048 sc1\n\t"
    "v_cvt_pk_bf16_f32 v245, v245, v249\n\t"
    "v_mfma_f32_16x16x32_bf16 v[72:75], a[232:235], a[104:107], v[72:75]\n\t"
    "v_cvt_pk_bf16_f32 v246, v246, v250\n\t"
    "v_cvt_pk_bf16_f32 v247, v247, v251\n\t"
    "v_mfma_f32_16x16x32_bf16 v[76:79], a[232:235], a[108:111], v[76:79]\n\t"
    "ds_write_b128 v2, v[244:247] offset:3072\n\t"
    "ds_read_b128 v[232:235], v10 offset:128\n\t"
    "v_mfma_f32_16x16x32_bf16 v[60:63], a[224:227], a[108:111], v[60:63]\n\t"
    "ds_read_b128 v[236:239], v10 offset:192\n\t"
    "ds_write_b128 v7, v[112:115] offset:0\n\t"
    "s_waitcnt vmcnt(8)\n\t"
    "v_mfma_f32_16x16x32_bf16 v[48:51], a[236:239], a[112:115], v[48:51]\n\t"
    "buffer_load_dwordx4 a[224:227], v1, %[sW], s40 offen offset:3072 sc1\n\t"
    "s_add_u32 s40, s40, 4096\n\t"
    "v_mfma_f32_16x16x32_bf16 v[64:67], a[240:243], a[112:115], v[64:67]\n\t"
    "ds_write_b128 v7, v[116:119] offset:1024\n\t"
    "ds_write_b128 v7, v[120:123] offset:2048\n\t"
    "v_mfma_f32_16x16x32_bf16 v[68:71], a[240:243], a[116:119], v[68:71]\n\t"
    "ds_write_b128 v7, v[124:127] offset:3072\n\t"
    "v_mfma_f32_16x16x32_bf16 v[52:55], a[236:239], a[116:119], v[52:55]\n\t"
    "v_mfma_f32_16x16x32_bf16 v[56:59], a[236:239], a[120:123], v[56:59]\n\t"
    "buffer_load_dwordx4 a[228:231], v1, %[sW], s40 offen offset:0 sc1\n\t"
    "v_mfma_f32_16x16x32_bf16 v[72:75], a[240:243], a[120:123], v[72:75]\n\t"
    "v_mfma_f32_16x16x32_bf16 v[76:79], a[240:243], a[124:127], v[76:79]\n\t"
    "buffer_load_dwordx4 a[232:235], v1, %[sW], s40 offen offset:1024 sc1\n\t"
    "v_mfma_f32_16x16x32_bf16 v[60:63], a[236:239], a[124:127], v[60:63]\n\t"
    "s_waitcnt vmcnt(9)\n\t"
    "v_mfma_f32_16x16x32_bf16 v[48:51], a[244:247], a[144:147], v[48:51]\n\t"
    "buffer_load_dwordx4 a[240:243], v1, %[sW], s40 offen offset:2048 sc1\n\t"
    "v_mfma_f32_16x16x32_bf16 v[64:67], a[248:251], a[144:147], v[64:67]\n\t"
    "v_mfma_f32_16x16x32_bf16 v[68:71], a[248:251], a[148:151], v[68:71]\n\t"
    "v_mfma_f32_16x16x32_bf16 v[52:55], a[244:247], a[148:151], v[52:55]\n\t"
    "v_mfma_f32_16x16x32_bf16 v[56:59], a[244:247], a[152:155], v[56:59]\n\t"
    "v_mfma_f32_16x16x32_bf16 v[72:75], a[248:251], a[152:155], v[72:75]\n\t"
    "v_mfma_f32_16x16x32_bf16 v[76:79], a[248:251], a[156:159], v[76:79]\n\t"
    "v_mfma_f32_16x16x32_bf16 v[60:63], a[244:247], a[156:159], v[60:63]\n\t"
    "s_waitcnt vmcnt(8)\n\t"
    "v_mfma_f32_16x16x32_bf16 v[48:51], a[252:255], a[160:163], v[48:51]\n\t"
    "buffer_load_dwordx4 a[244:247], v1, %[sW], s40 offen offset:3072 sc1\n\t"
    "s_add_u32 s40, s40, 4096\n\t"
    "v_mfma_f32_16x16x32_bf16 v[64:67], v[252:255], a[160:163], v[64:67]\n\t"
    "v_mfma_f32_16x16x32_bf16 v[68:71], v[252:255], a[164:167], v[68:71]\n\t"
    "v_mfma_f32_16x16x32_bf16 v[52:55], a[252:255], a[164:167], v[52:55]\n\t"
    "v_mfma_f32_16x16x32_bf16 v[56:59], a[252:255], a[168:171], v[56:59]\n\t"
    "buffer_load_dwordx4 a[248:251], v1, %[sW], s40 offen offset:0 sc1\n\t"
    "v_mfma_f32_16x16x32_bf16 v[72:75], v[252:255], a[168:171], v[72:75]\n\t"
    "v_mfma_f32_16x16x32_bf16 v[76:79], v[252:255], a[172:175], v[76:79]\n\t"
    "v_mfma_f32_16x16x32_bf16 v[60:63], a[252:255], a[172:175], v[60:63]\n\t"
    "s_waitcnt vmcnt(8)\n\t"
    "v_mfma_f32_16x16x32_bf16 v[48:51], a[208:211], a[192:195], v[48:51]\n\t"
    "buffer_load_dwordx4 a[252:255], v1, %[sW], s40 offen offset:1024 sc1\n\t"
    "v_mfma_f32_16x16x32_bf16 v[64:67], a[212:215], a[192:195], v[64:67]\n\t"
    "v_mfma_f32_16x16x32_bf16 v[68:71], a[212:215], a[196:199], v[68:71]\n\t"
    "v_mfma_f32_16x16x32_bf16 v[52:55], a[208:211], a[196:199], v[52:55]\n\t"
    "v_mfma_f32_16x16x32_bf16 v[56:59], a[208:211], a[200:203], v[56:59]\n\t"
    "buffer_load_dwordx4 v[252:255], v1, %[sW], s40 offen offset:2048 sc1\n\t"
    "v_mfma_f32_16x16x32_bf16 v[72:75], a[212:215], a[200:203], v[72:75]\n\t"
    "v_mfma_f32_16x16x32_bf16 v[76:79], a[212:215], a[204:207], v[76:79]\n\t"
    "v_mfma_f32_16x16x32_bf16 v[60:63], a[208:211], a[204:207], v[60:63]\n\t"
    "s_waitcnt vmcnt(8)\n\t"
    "v_mfma_f32_16x16x32_bf16 v[48:51], a[216:219], a[32:35], v[48:51]\n\t"
    "buffer_load_dwordx4 a[208:211], v1, %[sW], s40 offen offset:3072 sc1\n\t"
    "s_add_u32 s40, s40, 4096\n\t"
    "v_mfma_f32_16x16x32_bf16 v[64:67], a[220:223], a[32:35], v[64:67]\n\t"
    "v_mfma_f32_16x16x32_bf16 v[68:71], a[220:223], a[36:39], v[68:71]\n\t"
    "v_mfma_f32_16x16x32_bf16 v[52:55], a[216:219], a[36:39], v[52:55]\n\t"
    "v_mfma_f32_16x16x32_bf16 v[56:59], a[216:219], a[40:43], v[56:59]\n\t"
    "buffer_load_dwordx4 a[212:215], v1, %[sW], s40 offen offset:0 sc1\n\t"
    "v_mfma_f32_16x16x32_bf16 v[72:75], a[220:223], a[40:43], v[72:75]\n\t"
    "v_mfma_f32_16x16x32_bf16 v[76:79], a[220:223], a[44:47], v[76:79]\n\t"
    "v_mfma_f32_16x16x32_bf16 v[60:63], a[216:219], a[44:47], v[60:63]\n\t"
    "s_waitcnt vmcnt(8)\n\t"
    "v_mfma_f32_16x16x32_bf16 v[48:51], a[224:227], a[80:83], v[48:51]\n\t"
    "buffer_load_dwordx4 a[216:219], v1, %[sW], s40 offen offset:1024 sc1\n\t"
    "v_mfma_f32_16x16x32_bf16 v[64:67], a[228:231], a[80:83], v[64:67]\n\t"
    "v_mfma_f32_16x16x32_bf16 v[68:71], a[228:231], a[84:87], v[68:71]\n\t"
    "v_mfma_f32_16x16x32_bf16 v[52:55], a[224:227], a[84:87], v[52:55]\n\t"
    "v_mfma_f32_16x16x32_bf16 v[56:59], a[224:227], a[88:91], v[56:59]\n\t"
    "buffer_load_dwordx4 a[220:223], v1, %[sW], s40 offen offset:2048 sc1\n\t"
    "v_mfma_f32_16x16x32_bf16 v[72:75], a[228:231], a[88:91], v[72:75]\n\t"
    "v_mfma_f32_16x16x32_bf16 v[76:79], a[228:231], a[92:95], v[76:79]\n\t"
    "v_mfma_f32_16x16x32_bf16 v[60:63], a[224:227], a[92:95], v[60:63]\n\t"
    "s_waitcnt vmcnt(8)\n\t"
    "v_mfma_f32_16x16x32_bf16 v[48:51], a[232:235], a[128:131], v[48:51]\n\t"
    "buffer_load_dwordx4 a[224:227], v1, %[sW], s40 offen offset:3072 sc1\n\t"
    "s_add_u32 s40, s40, 4096\n\t"
    "v_mfma_f32_16x16x32_bf16 v[64:67], a[240:243], a[128:131], v[64:67]\n\t"
    "v_mfma_f32_16x16x32_bf16 v[68:71], a[240:243], a[132:135], v[68:71]\n\t"
    "v_mfma_f32_16x16x32_bf16 v[52:55], a[232:235], a[132:135], v[52:55]\n\t"
    "v_mfma_f32_16x16x32_bf16 v[56:59], a[232:235], a[136:139], v[56:59]\n\t"
    "buffer_load_dwordx4 a[228:231], v1, %[sW], s40 offen offset:0 sc1\n\t"
    "v_mfma_f32_16x16x32_bf16 v[72:75], a[240:243], a[136:139], v[72:75]\n\t"
    "v_mfma_f32_16x16x32_bf16 v[76:79], a[240:243], a[140:143], v[76:79]\n\t"
    "v_mfma_f32_16x16x32_bf16 v[60:63], a[232:235], a[140:143], v[60:63]\n\t"
    "s_waitcnt vmcnt(8)\n\t"
    "v_mfma_f32_16x16x32_bf16 v[48:51], a[244:247], a[176:179], v[48:51]\n\t"
    "buffer_load_dword a232, v11, %[sW], s40 offen sc1\n\t"
    "buffer_load_dwordx4 a[232:235], v1, %[sW], s40 offen offset:1024 sc1\n\t"
    "v_mfma_f32_16x16x32_bf16 v[64:67], a[248:251], a[176:179], v[64:67]\n\t"
    "v_mfma_f32_16x16x32_bf16 v[68:71], a[248:251], a[180:183], v[68:71]\n\t"
    "v_mfma_f32_16x16x32_bf16 v[52:55], a[244:247], a[180:183], v[52:55]\n\t"
    "v_mfma_f32_16x16x32_bf16 v[56:59], a[244:247], a[184:187], v[56:59]\n\t"
    "buffer_load_dwordx4 a[236:239], v1, %[sW], s40 offen offset:2048 sc1\n\t"
    "v_mfma_f32_16x16x32_bf16 v[72:75], a[248:251], a[184:187], v[72:75]\n\t"
    "v_mfma_f32_16x16x32_bf16 v[76:79], a[248:251], a[188:191], v[76:79]\n\t"
    "buffer_load_dwordx4 a[240:243], v1, %[sW], s40 offen offset:3072 sc1\n\t"
    "s_add_u32 s40, s40, 4096\n\t"
    "v_mfma_f32_16x16x32_bf16 v[60:63], a[244:247], a[188:191], v[60:63]\n\t"
    "s_waitcnt vmcnt(10)\n\t"
    "v_mfma_f32_16x16x32_bf16 v[80:83], a[252:255], a[0:3], v[80:83]\n\t"
    "buffer_load_dwordx4 a[248:251], v1, %[sW], s40 offen offset:0 sc1\n\t"
    "v_mfma_f32_16x16x32_bf16 v[96:99], v[252:255], a[0:3], v[96:99]\n\t"
    "v_mfma_f32_16x16x32_bf16 v[100:103], v[252:255], a[4:7], v[100:103]\n\t"
    "s_waitcnt lgkmcnt(5)\n\t"
    "v_add_f32_e32 v240, v48, v232\n\t"
    "v_add_f32_e32 v241, v49, v233\n\t"
    "v_mfma_f32_16x16x32_bf16 v[84:87], a[252:255], a[4:7], v[84:87]\n\t"
    "v_add_f32_e32 v242, v50, v234\n\t"
    "v_add_f32_e32 v243, v51, v235\n\t"
    "v_mfma_f32_16x16x32_bf16 v[88:91], a[252:255], a[8:11], v[88:91]\n\t"
    "s_waitcnt lgkmcnt(4)\n\t"
    "v_add_f32_e32 v248, v64, v236\n\t"
    "v_add_f32_e32 v249, v65, v237\n\t"
    "v_mfma_f32_16x16x32_bf16 v[104:107], v[252:255], a[8:11], v[104:107]\n\t"
    "v_add_f32_e32 v250, v66, v238\n\t"
    "v_add_f32_e32 v251, v67, v239\n\t"
    "v_mfma_f32_16x16x32_bf16 v[108:111], v[252:255], a[12:15], v[108:111]\n\t"
    "v_mul_f32_e32 v240, v240, v160\n\t"
    "v_mul_f32_e32 v241, v241, v161\n\t"
    "v_mfma_f32_16x16x32_bf16 v[92:95], a[252:255], a[12:15], v[92:95]\n\t"
    "v_mul_f32_e32 v242, v242, v162\n\t"
    "v_mul_f32_e32 v243, v243, v163\n\t"
    "s_waitcnt vmcnt(9)\n\t"
    "v_mfma_f32_16x16x32_bf16 v[80:83], a[208:211], a[16:19], v[80:83]\n\t"
    "buffer_load_dwordx4 a[252:255], v1, %[sW], s40 offen offset:1024 sc1\n\t"
    "v_mul_f32_e32 v248, v248, v176\n\t"
    "v_mfma_f32_16x16x32_bf16 v[96:99], a[212:215], a[16:19], v[96:99]\n\t"
    "v_mul_f32_e32 v249, v249, v177\n\t"
    "v_mul_f32_e32 v250, v250, v178\n\t"
    "v_mfma_f32_16x16x32_bf16 v[100:103], a[212:215], a[20:23], v[100:103]\n\t"
    "v_mul_f32_e32 v251, v251, v179\n\t"
    "v_cvt_pk_bf16_f32 v240, v240, v248\n\t"
    "v_mfma_f32_16x16x32_bf16 v[84:87], a[208:211], a[20:23], v[84:87]\n\t"
    "v_cvt_pk_bf16_f32 v241, v241, v249\n\t"
    "v_cvt_pk_bf16_f32 v242, v242, v250\n\t"
    "v_mfma_f32_16x16x32_bf16 v[88:91], a[208:211], a[24:27], v[88:91]\n\t"
    "buffer_load_dwordx4 v[252:255], v1, %[sW], s40 offen offset:2048 sc1\n\t"
    "v_cvt_pk_bf16_f32 v243, v243, v251\n\t"
    "v_mfma_f32_16x16x32_bf16 v[104:107], a[212:215], a[24:27], v[104:107]\n\t"
    "ds_write_b128 v2, v[240:243] offset:4096\n\t"
    "v_add_f32_e32 v244, v52, v232\n\t"
    "v_mfma_f32_16x16x32_bf16 v[108:111], a[212:215], a[28:31], v[108:111]\n\t"
    "v_add_f32_e32 v245, v53, v233\n\t"
    "v_add_f32_e32 v246, v54, v234\n\t"
    "v_mfma_f32_16x16x32_bf16 v[92:95], a[208:211], a[28:31], v[92:95]\n\t"
    "v_add_f32_e32 v247, v55, v235\n\t"
    "v_add_f32_e32 v248, v68, v236\n\t"
    "s_waitcnt vmcnt(9)\n\t"
    "v_mfma_f32_16x16x32_bf16 v[80:83], a[216:219], a[48:51], v[80:83]\n\t"
    "buffer_load_dwordx4 a[208:211], v1, %[sW], s40 offen offset:3072 sc1\n\t"
    "s_add_u32 s40, s40, 4096\n\t"
    "v_mfma_f32_16x16x32_bf16 v[96:99], a[220:223], a[48:51], v[96:99]\n\t"
    "v_add_f32_e32 v249, v69, v237\n\t"
    "v_add_f32_e32 v250, v70, v238\n\t"
    "v_mfma_f32_16x16x32_bf16 v[100:103], a[220:223], a[52:55], v[100:103]\n\t"
    "v_add_f32_e32 v251, v71, v239\n\t"
    "v_mul_f32_e32 v244, v244, v164\n\t"
    "v_mfma_f32_16x16x32_bf16 v[84:87], a[216:219], a[52:55], v[84:87]\n\t"
    "v_mul_f32_e32 v245, v245, v165\n\t"
    "v_mul_f32_e32 v246, v246, v166\n\t"
    "v_mfma_f32_16x16x32_bf16 v[88:91], a[216:219], a[56:59], v[88:91]\n\t"
    "buffer_load_dwordx4 a[212:215], v1, %[sW], s40 offen offset:0 sc1\n\t"
    "v_mul_f32_e32 v247, v247, v167\n\t"
    "v_mfma_f32_16x16x32_bf16 v[104:107], a[220:223], a[56:59], v[104:107]\n\t"
    "v_mul_f32_e32 v248, v248, v180\n\t"
    "v_mul_f32_e32 v249, v249, v181\n\t"
    "v_mfma_f32_16x16x32_bf16 v[108:111], a[220:223], a[60:63], v[108:111]\n\t"
    "v_mul_f32_e32 v250, v250, v182\n\t"
    "v_mul_f32_e32 v251, v251, v183\n\t"
    "v_mfma_f32_16x16x32_bf16 v[92:95], a[216:219], a[60:63], v[92:95]\n\t"
    "v_cvt_pk_bf16_f32 v244, v244, v248\n\t"
    "v_cvt_pk_bf16_f32 v245, v245, v249\n\t"
    "s_waitcnt vmcnt(9)\n\t"
    "v_mfma_f32_16x16x32_bf16 v[80:83], a[224:227], a[64:67], v[80:83]\n\t"
    "buffer_load_dwordx4 a[216:219], v1, %[sW], s40 offen offset:1024 sc1\n\t"
    "v_cvt_pk_bf16_f32 v246, v246, v250\n\t"
    "v_mfma_f32_16x16x32_bf16 v[96:99], a[228:231], a[64:67], v[96:99]\n\t"
    "v_cvt_pk_bf16_f32 v247, v247, v251\n\t"
    "ds_write_b128 v2, v[244:247] offset:5120\n\t"
    "v_mfma_f32_16x16x32_bf16 v[100:103], a[228:231], a[68:71], v[100:103]\n\t"
    "v_add_f32_e32 v240, v56, v232\n\t"
    "v_add_f32_e32 v241, v57, v233\n\t"
    "v_mfma_f32_16x16x32_bf16 v[84:87], a[224:227], a[68:71], v[84:87]\n\t"
    "v_add_f32_e32 v242, v58, v234\n\t"
    "v_add_f32_e32 v243, v59, v235\n\t"
    "v_mfma_f32_16x16x32_bf16 v[88:91], a[224:227], a[72:75], v[88:91]\n\t"
    "buffer_load_dwordx4 a[220:223], v1, %[sW], s40 offen offset:2048 sc1\n\t"
    "v_add_f32_e32 v248, v72, v236\n\t"
    "v_mfma_f32_16x16x32_bf16 v[104:107], a[228:231], a[72:75], v[104:107]\n\t"
    "v_add_f32_e32 v249, v73, v237\n\t"
    "v_add_f32_e32 v250, v74, v238\n\t"
    "v_mfma_f32_16x16x32_bf16 v[108:111], a[228:231], a[76:79], v[108:111]\n\t"
    "v_add_f32_e32 v251, v75, v239\n\t"
    "v_mul_f32_e32 v240, v240, v168\n\t"
    "v_mfma_f32_16x16x32_bf16 v[92:95], a[224:227], a[76:79], v[92:95]\n\t"
    "v_mul_f32_e32 v241, v241, v169\n\t"
    "v_mul_f32_e32 v242, v242, v170\n\t"
    "s_waitcnt vmcnt(8)\n\t"
    "v_mfma_f32_16x16x32_bf16 v[80:83], a[232:235], a[96:99], v[80:83]\n\t"
    "buffer_load_dwordx4 a[224:227], v1, %[sW], s40 offen offset:3072 sc1\n\t"
    "s_add_u32 s40, s40, 4096\n\t"
    "v_mfma_f32_16x16x32_bf16 v[96:99], a[236:239], a[96:99], v[96:99]\n\t"
    "v_mul_f32_e32 v243, v243, v171\n\t"
    "v_mul_f32_e32 v248, v248, v184\n\t"
    "v_mfma_f32_16x16x32_bf16 v[100:103], a[236:239], a[100:103], v[100:103]\n\t"
    "v_mul_f32_e32 v249, v249, v185\n\t"
    "v_mul_f32_e32 v250, v250, v186\n\t"
    "v_mfma_f32_16x16x32_bf16 v[84:87], a[232:235], a[100:103], v[84:87]\n\t"
    "v_mul_f32_e32 v251, v251, v187\n\t"
    "v_cvt_pk_bf16_f32 v240, v240, v248\n\t"
    "v_mfma_f32_16x16x32_bf16 v[88:91], a[232:235], a[104:107], v[88:91]\n\t"
    "buffer_load_dwordx4 a[228:231], v1, %[sW], s40 offen offset:0 sc1\n\t"
    "v_cvt_pk_bf16_f32 v241, v241, v249\n\t"
    "v_mfma_f32_16x16x32_bf16 v[104:107], a[236:239], a[104:107], v[104:107]\n\t"
    "v_cvt_pk_bf16_f32 v242, v242, v250\n\t"
    "v_cvt_pk_bf16_f32 v243, v243, v251\n\t"
    "v_mfma_f32_16x16x32_bf16 v[108:111], a[236:239], a[108:111], v[108:111]\n\t"
    "ds_write_b128 v2, v[240:243] offset:6144\n\t"
    "v_add_f32_e32 v244, v60, v232\n\t"
    "v_mfma_f32_16x16x32_bf16 v[92:95], a[232:235], a[108:111], v[92:95]\n\t"
    "v_add_f32_e32 v245, v61, v233\n\t"
    "v_add_f32_e32 v246, v62, v234\n\t"
    "s_waitcnt vmcnt(8)\n\t"
    "v_mfma_f32_16x16x32_bf16 v[80:83], a[240:243], a[112:115], v[80:83]\n\t"
    "buffer_load_dwordx4 a[232:235], v1, %[sW], s40 offen offset:1024 sc1\n\t"
    "v_add_f32_e32 v247, v63, v235\n\t"
    "v_mfma_f32_16x16x32_bf16 v[96:99], a[248:251], a[112:115], v[96:99]\n\t"
    "v_add_f32_e32 v248, v76, v236\n\t"
    "v_add_f32_e32 v249, v77, v237\n\t"
    "v_mfma_f32_16x16x32_bf16 v[100:103], a[248:251], a[116:119], v[100:103]\n\t"
    "v_add_f32_e32 v250, v78, v238\n\t"
    "v_add_f32_e32 v251, v79, v239\n\t"
    "v_mfma_f32_16x16x32_bf16 v[84:87], a[240:243], a[116:119], v[84:87]\n\t"
    "v_mul_f32_e32 v244, v244, v172\n\t"
    "v_mul_f32_e32 v245, v245, v173\n\t"
    "v_mfma_f32_16x16x32_bf16 v[88:91], a[240:243], a[120:123], v[88:91]\n\t"
    "buffer_load_dwordx4 a[236:239], v1, %[sW], s40 offen offset:2048 sc1\n\t"
    "v_mul_f32_e32 v246, v246, v174\n\t"
    "v_mfma_f32_16x16x32_bf16 v[104:107], a[248:251], a[120:123], v[104:107]\n\t"
    "v_mul_f32_e32 v247, v247, v175\n\t"
    "v_mul_f32_e32 v248, v248, v188\n\t"
    "v_mfma_f32_16x16x32_bf16 v[108:111], a[248:251], a[124:127], v[108:111]\n\t"
    "v_mul_f32_e32 v249, v249, v189\n\t"
    "v_mul_f32_e32 v250, v250, v190\n\t"
    "v_mfma_f32_16x16x32_bf16 v[92:95], a[240:243], a[124:127], v[92:95]\n\t"
    "v_mul_f32_e32 v251, v251, v191\n\t"
    "v_cvt_pk_bf16_f32 v244, v244, v248\n\t"
    "s_waitcnt vmcnt(8)\n\t"
    "v_mfma_f32_16x16x32_bf16 v[80:83], a[252:255], a[144:147], v[80:83]\n\t"
    "buffer_load_dwordx4 a[240:243], v1, %[sW], s40 offen offset:3072 sc1\n\t"
    "s_add_u32 s40, s40, 4096\n\t"
    "v_mfma_f32_16x16x32_bf16 v[96:99], v[252:255], a[144:147], v[96:99]\n\t"
    "v_cvt_pk_bf16_f32 v245, v245, v249\n\t"
    "v_cvt_pk_bf16_f32 v246, v246, v250\n\t"
    "v_mfma_f32_16x16x32_bf16 v[100:103], v[252:255], a[148:151], v[100:103]\n\t"
    "v_cvt_pk_bf16_f32 v247, v247, v251\n\t"
    "ds_write_b128 v2, v[244:247] offset:7168\n\t"
    "v_mfma_f32_16x16x32_bf16 v[84:87], a[252:255], a[148:151], v[84:87]\n\t"
    "ds_read_b128 v[232:235], v10 offset:384\n\t"
    "ds_read_b128 v[236:239], v10 offset:320\n\t"
    "v_mfma_f32_16x16x32_bf16 v[88:91], a[252:255], a[152:155], v[88:91]\n\t"
    "buffer_load_dwordx4 a[244:247], v1, %[sW], s40 offen offset:0 sc1\n\t"
    "s_waitcnt lgkmcnt(0)\n\t"
    "v_mfma_f32_16x16x32_bf16 v[104:107], v[252:255], a[152:155], v[104:107]\n\t"
    "s_barrier\n\t"
    "ds_read_b128 v[248:251], v8 offset:0\n\t"
    "v_mfma_f32_16x16x32_bf16 v[108:111], v[252:255], a[156:159], v[108:111]\n\t"
    "buffer_load_dwordx4 a[248:251], v1, %[sW], s40 offen offset:1024 sc1\n\t"
    "ds_read_b128 v[12:15], v8 offset:4096\n\t"
    "v_mfma_f32_16x16x32_bf16 v[92:95], a[252:255], a[156:159], v[92:95]\n\t"
    "ds_read_b128 a[0:3], v2 offset:0\n\t"
    "ds_read_b128 a[4:7], v2 offset:1024\n\t"
    "s_waitcnt vmcnt(9)\n\t"
    "v_mfma_f32_16x16x32_bf16 v[80:83], a[208:211], a[160:163], v[80:83]\n\t"
    "buffer_load_dwordx4 v[252:255], v1, %[sW], s40 offen offset:2048 sc1\n\t"
    "ds_read_b128 a[8:11], v2 offset:2048\n\t"
    "v_mfma_f32_16x16x32_bf16 v[96:99], a[212:215], a[160:163], v[96:99]\n\t"
    "ds_read_b128 a[12:15], v2 offset:3072\n\t"
    "ds_read_b128 a[16:19], v2 offset:4096\n\t"
    "v_mfma_f32_16x16x32_bf16 v[100:103], a[212:215], a[164:167], v[100:103]\n\t"
    "ds_read_b128 a[20:23], v2 offset:5120\n\t"
    "ds_read_b128 a[24:27], v2 offset:6144\n\t"
    "v_mfma_f32_16x16x32_bf16 v[84:87], a[208:211], a[164:167], v[84:87]\n\t"
    "ds_read_b128 a[28:31], v2 offset:7168\n\t"
    "s_waitcnt lgkmcnt(8)\n\t"
    "v_add_f32_e32 v240, v248, v12\n\t"
    "v_mfma_f32_16x16x32_bf16 v[88:91], a[208:211], a[168:171], v[88:91]\n\t"
    "v_add_f32_e32 v241, v249, v13\n\t"
    "v_add_f32_e32 v242, v250, v14\n\t"
    "v_mfma_f32_16x16x32_bf16 v[104:107], a[212:215], a[168:171], v[104:107]\n\t"
    "v_add_f32_e32 v243, v251, v15\n\t"
    "v_add_f32_e32 v240, v240, v232\n\t"
    "v_mfma_f32_16x16x32_bf16 v[108:111], a[212:215], a[172:175], v[108:111]\n\t"
    "v_add_f32_e32 v241, v241, v233\n\t"
    "v_add_f32_e32 v242, v242, v234\n\t"
    "v_mfma_f32_16x16x32_bf16 v[92:95], a[208:211], a[172:175], v[92:95]\n\t"
    "v_add_f32_e32 v243, v243, v235\n\t"
    "v_mul_f32_e32 v240, v240, v224\n\t"
    "s_waitcnt vmcnt(8)\n\t"
    "v_mfma_f32_16x16x32_bf16 v[80:83], a[216:219], a[192:195], v[80:83]\n\t"
    "buffer_load_dwordx4 a[208:211], v1, %[sW], s40 offen offset:3072 sc1\n\t"
    "s_add_u32 s40, s40, 4096\n\t"
    "v_mfma_f32_16x16x32_bf16 v[96:99], a[220:223], a[192:195], v[96:99]\n\t"
    "v_mul_f32_e32 v241, v241, v225\n\t"
    "v_mul_f32_e32 v242, v242, v226\n\t"
    "v_mfma_f32_16x16x32_bf16 v[100:103], a[220:223], a[196:199], v[100:103]\n\t"
    "v_mul_f32_e32 v243, v243, v227\n\t"
    "ds_read_b128 v[232:235], v10 offset:448\n\t"
    "v_mfma_f32_16x16x32_bf16 v[84:87], a[216:219], a[196:199], v[84:87]\n\t"
    "ds_read_b128 v[248:251], v8 offset:8192\n\t"
    "ds_read_b128 v[12:15], v8 offset:12288\n\t"
    "v_mfma_f32_16x16x32_bf16 v[88:91], a[216:219], a[200:203], v[88:91]\n\t"
    "buffer_load_dwordx4 a[212:215], v1, %[sW], s40 offen offset:0 sc1\n\t"
    "ds_read_b128 a[48:51], v3 offset:0\n\t"
    "v_mfma_f32_16x16x32_bf16 v[104:107], a[220:223], a[200:203], v[104:107]\n\t"
    "ds_read_b128 a[52:55], v3 offset:1024\n\t"
    "ds_read_b128 a[56:59], v3 offset:2048\n\t"
    "v_mfma_f32_16x16x32_bf16 v[108:111], a[220:223], a[204:207], v[108:111]\n\t"
    "ds_read_b128 a[60:63], v3 offset:3072\n\t"
    "ds_read_b128 a[64:67], v3 offset:4096\n\t"
    "v_mfma_f32_16x16x32_bf16 v[92:95], a[216:219], a[204:207], v[92:95]\n\t"
    "ds_read_b128 a[68:71], v3 offset:5120\n\t"
    "ds_read_b128 a[72:75], v3 offset:6144\n\t"
    "s_waitcnt vmcnt(8)\n\t"
    "v_mfma_f32_16x16x32_bf16 v[80:83], a[224:227], a[32:35], v[80:83]\n\t"
    "buffer_load_dwordx4 a[216:219], v1, %[sW], s40 offen offset:1024 sc1\n\t"
    "ds_read_b128 a[76:79], v3 offset:7168\n\t"
    "v_mfma_f32_16x16x32_bf16 v[96:99], a[228:231], a[32:35], v[96:99]\n\t"
    "s_waitcnt lgkmcnt(8)\n\t"
    "v_add_f32_e32 v248, v248, v12\n\t"
    "v_add_f32_e32 v249, v249, v13\n\t"
    "v_mfma_f32_16x16x32_bf16 v[100:103], a[228:231], a[36:39], v[100:103]\n\t"
    "v_add_f32_e32 v250, v250, v14\n\t"
    "v_add_f32_e32 v251, v251, v15\n\t"
    "v_mfma_f32_16x16x32_bf16 v[84:87], a[224:227], a[36:39], v[84:87]\n\t"
    "v_add_f32_e32 v248, v248, v232\n\t"
    "v_add_f32_e32 v249, v249, v233\n\t"
    "v_mfma_f32_16x16x32_bf16 v[88:91], a[224:227], a[40:43], v[88:91]\n\t"
    "buffer_load_dwordx4 a[220:223], v1, %[sW], s40 offen offset:2048 sc1\n\t"
    "v_add_f32_e32 v250, v250, v234\n\t"
    "v_mfma_f32_16x16x32_bf16 v[104:107], a[228:231], a[40:43], v[104:107]\n\t"
    "v_add_f32_e32 v251, v251, v235\n\t"
    "v_mul_f32_e32 v248, v248, v228\n\t"
    "v_mfma_f32_16x16x32_bf16 v[108:111], a[228:231], a[44:47], v[108:111]\n\t"
    "v_mul_f32_e32 v249, v249, v229\n\t"
    "v_mul_f32_e32 v250, v250, v230\n\t"
    "v_mfma_f32_16x16x32_bf16 v[92:95], a[224:227], a[44:47], v[92:95]\n\t"
    "v_mul_f32_e32 v251, v251, v231\n\t"
    "v_cvt_pk_bf16_f32 v240, v240, v248\n\t"
    "s_waitcnt vmcnt(8)\n\t"
    "v_mfma_f32_16x16x32_bf16 v[80:83], a[232:235], a[80:83], v[80:83]\n\t"
    "buffer_load_dwordx4 a[224:227], v1, %[sW], s40 offen offset:3072 sc1\n\t"
    "s_add_u32 s40, s40, 4096\n\t"
    "v_mfma_f32_16x16x32_bf16 v[96:99], a[236:239], a[80:83], v[96:99]\n\t"
    "v_cvt_pk_bf16_f32 v241, v241, v249\n\t"
    "v_cvt_pk_bf16_f32 v242, v242, v250\n\t"
    "v_mfma_f32_16x16x32_bf16 v[100:103], a[236:239], a[84:87], v[100:103]\n\t"
    "v_cvt_pk_bf16_f32 v243, v243, v251\n\t"
    "ds_write_b128 v9, v[240:243] offset:0\n\t"
    "v_mfma_f32_16x16x32_bf16 v[84:87], a[232:235], a[84:87], v[84:87]\n\t"
    "ds_read_b128 v[232:235], v10 offset:256\n\t"
    "ds_read_b128 a[96:99], v4 offset:0\n\t"
    "v_mfma_f32_16x16x32_bf16 v[88:91], a[232:235], a[88:91], v[88:91]\n\t"
    "buffer_load_dwordx4 a[228:231], v1, %[sW], s40 offen offset:0 sc1\n\t"
    "ds_read_b128 a[100:103], v4 offset:1024\n\t"
    "v_mfma_f32_16x16x32_bf16 v[104:107], a[236:239], a[88:91], v[104:107]\n\t"
    "ds_read_b128 a[104:107], v4 offset:2048\n\t"
    "ds_read_b128 a[108:111], v4 offset:3072\n\t"
    "v_mfma_f32_16x16x32_bf16 v[108:111], a[236:239], a[92:95], v[108:111]\n\t"
    "ds_read_b128 a[112:115], v4 offset:4096\n\t"
    "ds_read_b128 a[116:119], v4 offset:5120\n\t"
    "v_mfma_f32_16x16x32_bf16 v[92:95], a[232:235], a[92:95], v[92:95]\n\t"
    "ds_read_b128 a[120:123], v4 offset:6144\n\t"
    "ds_read_b128 a[124:127], v4 offset:7168\n\t"
    "s_waitcnt vmcnt(8)\n\t"
    "v_mfma_f32_16x16x32_bf16 v[80:83], a[240:243], a[128:131], v[80:83]\n\t"
    "buffer_load_dwordx4 a[232:235], v1, %[sW], s40 offen offset:1024 sc1\n\t"
    "ds_read_b128 a[144:147], v5 offset:0\n\t"
    "v_mfma_f32_16x16x32_bf16 v[96:99], a[244:247], a[128:131], v[96:99]\n\t"
    "ds_read_b128 a[148:151], v5 offset:1024\n\t"
    "ds_read_b128 a[152:155], v5 offset:2048\n\t"
    "v_mfma_f32_16x16x32_bf16 v[100:103], a[244:247], a[132:135], v[100:103]\n\t"
    "ds_read_b128 a[156:159], v5 offset:3072\n\t"
    "ds_read_b128 a[160:163], v5 offset:4096\n\t"
    "v_mfma_f32_16x16x32_bf16 v[84:87], a[240:243], a[132:135], v[84:87]\n\t"
    "ds_read_b128 a[164:167], v5 offset:5120\n\t"
    "ds_read_b128 a[168:171], v5 offset:6144\n\t"
    "v_mfma_f32_16x16x32_bf16 v[88:91], a[240:243], a[136:139], v[88:91]\n\t"
    "buffer_load_dwordx4 a[236:239], v1, %[sW], s40 offen offset:2048 sc1\n\t"
    "ds_read_b128 a[172:175], v5 offset:7168\n\t"
    "v_mfma_f32_16x16x32_bf16 v[104:107], a[244:247], a[136:139], v[104:107]\n\t"
    "v_add_u32_e32 v10, 2048, v10\n\t"
    "v_mfma_f32_16x16x32_bf16 v[108:111], a[244:247], a[140:143], v[108:111]\n\t"
    "v_mfma_f32_16x16x32_bf16 v[92:95], a[240:243], a[140:143], v[92:95]\n\t"
    "s_waitcnt vmcnt(8)\n\t"
    "v_mfma_f32_16x16x32_bf16 v[80:83], a[248:251], a[176:179], v[80:83]\n\t"
    "buffer_load_dwordx4 a[240:243], v1, %[sW], s40 offen offset:3072 sc1\n\t"
    "s_add_u32 s40, s40, 4096\n\t"
    "v_mfma_f32_16x16x32_bf16 v[96:99], v[252:255], a[176:179], v[96:99]\n\t"
    "v_mfma_f32_16x16x32_bf16 v[100:103], v[252:255], a[180:183], v[100:103]\n\t"
    "v_mfma_f32_16x16x32_bf16 v[84:87], a[248:251], a[180:183], v[84:87]\n\t"
    "v_mfma_f32_16x16x32_bf16 v[88:91], a[248:251], a[184:187], v[88:91]\n\t"
    "buffer_load_dwordx4 a[244:247], v1, %[sW], s40 offen offset:0 sc1\n\t"
    "s_add_u32 s40, s40, 1024\n\t"
    "v_mfma_f32_16x16x32_bf16 v[104:107], v[252:255], a[184:187], v[104:107]\n\t"
    "v_mfma_f32_16x16x32_bf16 v[108:111], v[252:255], a[188:191], v[108:111]\n\t"
    "v_mfma_f32_16x16x32_bf16 v[92:95], a[248:251], a[188:191], v[92:95]\n\t"
    "s_sub_u32 s41, s41, 1\n\t"
    "s_cmp_lg_u32 s41, 0\n\t"
    "s_cbranch_scc1 1b\n\t"
    "s_waitcnt vmcnt(8)\n\t"
    "v_mfma_f32_16x16x32_bf16 v[16:19], a[208:211], a[0:3], v[16:19]\n\t"
    "buffer_load_dwordx4 a[248:251], v1, %[sW], s40 offen offset:0 sc1\n\t"
    "s_waitcnt lgkmcnt(15)\n\t"
    "v_add_f32_e32 v240, v80, v232\n\t"
    "v_mfma_f32_16x16x32_bf16 v[32:35], a[212:215], a[0:3], v[32:35]\n\t"
    "v_add_f32_e32 v241, v81, v233\n\t"
    "v_add_f32_e32 v242, v82, v234\n\t"
    "v_mfma_f32_16x16x32_bf16 v[36:39], a[212:215], a[4:7], v[36:39]\n\t"
    "v_add_f32_e32 v243, v83, v235\n\t"
    "v_add_f32_e32 v248, v96, v236\n\t"
    "v_mfma_f32_16x16x32_bf16 v[20:23], a[208:211], a[4:7], v[20:23]\n\t"
    "v_add_f32_e32 v249, v97, v237\n\t"
    "v_add_f32_e32 v250, v98, v238\n\t"
    "v_mfma_f32_16x16x32_bf16 v[24:27], a[208:211], a[8:11], v[24:27]\n\t"
    "buffer_load_dwordx4 a[252:255], v1, %[sW], s40 offen offset:1024 sc1\n\t"
    "v_add_f32_e32 v251, v99, v239\n\t"
    "v_mfma_f32_16x16x32_bf16 v[40:43], a[212:215], a[8:11], v[40:43]\n\t"
    "v_mul_f32_e32 v240, v240, v192\n\t"
    "v_mul_f32_e32 v241, v241, v193\n\t"
    "v_mfma_f32_16x16x32_bf16 v[44:47], a[212:215], a[12:15], v[44:47]\n\t"
    "buffer_load_dwordx4 v[252:255], v1, %[sW], s40 offen offset:2048 sc1\n\t"
    "v_mul_f32_e32 v242, v242, v194\n\t"
    "v_mfma_f32_16x16x32_bf16 v[28:31], a[208:211], a[12:15], v[28:31]\n\t"
    "v_mul_f32_e32 v243, v243, v195\n\t"
    "v_mul_f32_e32 v248, v248, v208\n\t"
    "s_waitcnt vmcnt(9)\n\t"
    "v_mfma_f32_16x16x32_bf16 v[16:19], a[216:219], a[16:19], v[16:19]\n\t"
    "buffer_load_dwordx4 a[208:211], v1, %[sW], s40 offen offset:3072 sc1\n\t"
    "s_add_u32 s40, s40, 4096\n\t"
    "v_mfma_f32_16x16x32_bf16 v[32:35], a[220:223], a[16:19], v[32:35]\n\t"
    "v_mul_f32_e32 v249, v249, v209\n\t"
    "v_mul_f32_e32 v250, v250, v210\n\t"
    "v_mfma_f32_16x16x32_bf16 v[36:39], a[220:223], a[20:23], v[36:39]\n\t"
    "v_mul_f32_e32 v251, v251, v211\n\t"
    "v_cvt_pk_bf16_f32 v240, v240, v248\n\t"
    "v_mfma_f32_16x16x32_bf16 v[20:23], a[216:219], a[20:23], v[20:23]\n\t"
    "v_cvt_pk_bf16_f32 v241, v241, v249\n\t"
    "v_cvt_pk_bf16_f32 v242, v242, v250\n\t"
    "v_mfma_f32_16x16x32_bf16 v[24:27], a[216:219], a[24:27], v[24:27]\n\t"
    "v_cvt_pk_bf16_f32 v243, v243, v251\n\t"
    "ds_write_b128 v2, v[240:243] offset:8192\n\t"
    "v_mfma_f32_16x16x32_bf16 v[40:43], a[220:223], a[24:27], v[40:43]\n\t"
    "v_add_f32_e32 v244, v84, v232\n\t"
    "v_add_f32_e32 v245, v85, v233\n\t"
    "v_mfma_f32_16x16x32_bf16 v[44:47], a[220:223], a[28:31], v[44:47]\n\t"
    "v_add_f32_e32 v246, v86, v234\n\t"
    "v_add_f32_e32 v247, v87, v235\n\t"
    "v_mfma_f32_16x16x32_bf16 v[28:31], a[216:219], a[28:31], v[28:31]\n\t"
    "v_add_f32_e32 v248, v100, v236\n\t"
    "v_add_f32_e32 v249, v101, v237\n\t"
    "s_waitcnt vmcnt(8)\n\t"
    "v_mfma_f32_16x16x32_bf16 v[16:19], a[224:227], a[48:51], v[16:19]\n\t"
    "buffer_load_dwordx4 a[216:219], v1, %[sW], s40 offen offset:0 sc1\n\t"
    "v_add_f32_e32 v250, v102, v238\n\t"
    "v_mfma_f32_16x16x32_bf16 v[32:35], a[228:231], a[48:51], v[32:35]\n\t"
    "v_add_f32_e32 v251, v103, v239\n\t"
    "v_mul_f32_e32 v244, v244, v196\n\t"
    "v_mfma_f32_16x16x32_bf16 v[36:39], a[228:231], a[52:55], v[36:39]\n\t"
    "v_mul_f32_e32 v245, v245, v197\n\t"
    "v_mul_f32_e32 v246, v246, v198\n\t"
    "v_mfma_f32_16x16x32_bf16 v[20:23], a[224:227], a[52:55], v[20:23]\n\t"
    "v_mul_f32_e32 v247, v247, v199\n\t"
    "v_mul_f32_e32 v248, v248, v212\n\t"
    "v_mfma_f32_16x16x32_bf16 v[24:27], a[224:227], a[56:59], v[24:27]\n\t"
    "buffer_load_dwordx4 a[220:223], v1, %[sW], s40 offen offset:1024 sc1\n\t"
    "v_mul_f32_e32 v249, v249, v213\n\t"
    "v_mfma_f32_16x16x32_bf16 v[40:43], a[228:231], a[56:59], v[40:43]\n\t"
    "v_mul_f32_e32 v250, v250, v214\n\t"
    "v_mul_f32_e32 v251, v251, v215\n\t"
    "v_mfma_f32_16x16x32_bf16 v[44:47], a[228:231], a[60:63], v[44:47]\n\t"
    "v_cvt_pk_bf16_f32 v244, v244, v248\n\t"
    "v_cvt_pk_bf16_f32 v245, v245, v249\n\t"
    "v_mfma_f32_16x16x32_bf16 v[28:31], a[224:227], a[60:63], v[28:31]\n\t"
    "v_cvt_pk_bf16_f32 v246, v246, v250\n\t"
    "v_cvt_pk_bf16_f32 v247, v247, v251\n\t"
    "s_waitcnt vmcnt(8)\n\t"
    "v_mfma_f32_16x16x32_bf16 v[16:19], a[232:235], a[64:67], v[16:19]\n\t"
    "buffer_load_dwordx4 a[224:227], v1, %[sW], s40 offen offset:2048 sc1\n\t"
    "ds_write_b128 v2, v[244:247] offset:9216\n\t"
    "v_mfma_f32_16x16x32_bf16 v[32:35], a[236:239], a[64:67], v[32:35]\n\t"
    "v_add_f32_e32 v240, v88, v232\n\t"
    "v_add_f32_e32 v241, v89, v233\n\t"
    "v_mfma_f32_16x16x32_bf16 v[36:39], a[236:239], a[68:71], v[36:39]\n\t"
    "v_add_f32_e32 v242, v90, v234\n\t"
    "v_add_f32_e32 v243, v91, v235\n\t"
    "v_mfma_f32_16x16x32_bf16 v[20:23], a[232:235], a[68:71], v[20:23]\n\t"
    "v_add_f32_e32 v248, v104, v236\n\t"
    "v_add_f32_e32 v249, v105, v237\n\t"
    "v_mfma_f32_16x16x32_bf16 v[24:27], a[232:235], a[72:75], v[24:27]\n\t"
    "buffer_load_dwordx4 a[228:231], v1, %[sW], s40 offen offset:3072 sc1\n\t"
    "s_add_u32 s40, s40, 4096\n\t"
    "v_mfma_f32_16x16x32_bf16 v[40:43], a[236:239], a[72:75], v[40:43]\n\t"
    "v_add_f32_e32 v250, v106, v238\n\t"
    "v_add_f32_e32 v251, v107, v239\n\t"
    "v_mfma_f32_16x16x32_bf16 v[44:47], a[236:239], a[76:79], v[44:47]\n\t"
    "v_mul_f32_e32 v240, v240, v200\n\t"
    "v_mul_f32_e32 v241, v241, v201\n\t"
    "v_mfma_f32_16x16x32_bf16 v[28:31], a[232:235], a[76:79], v[28:31]\n\t"
    "v_mul_f32_e32 v242, v242, v202\n\t"
    "v_mul_f32_e32 v243, v243, v203\n\t"
    "s_waitcnt vmcnt(8)\n\t"
    "v_mfma_f32_16x16x32_bf16 v[16:19], a[240:243], a[96:99], v[16:19]\n\t"
    "buffer_load_dwordx4 a[232:235], v1, %[sW], s40 offen offset:0 sc1\n\t"
    "v_mul_f32_e32 v248, v248, v216\n\t"
    "v_mfma_f32_16x16x32_bf16 v[32:35], a[244:247], a[96:99], v[32:35]\n\t"
    "v_mul_f32_e32 v249, v249, v217\n\t"
    "v_mul_f32_e32 v250, v250, v218\n\t"
    "s_waitcnt lgkmcnt(15)\n\t"
    "v_mfma_f32_16x16x32_bf16 v[36:39], a[244:247], a[100:103], v[36:39]\n\t"
    "v_mul_f32_e32 v251, v251, v219\n\t"
    "v_cvt_pk_bf16_f32 v240, v240, v248\n\t"
    "v_mfma_f32_16x16x32_bf16 v[20:23], a[240:243], a[100:103], v[20:23]\n\t"
    "v_cvt_pk_bf16_f32 v241, v241, v249\n\t"
    "v_cvt_pk_bf16_f32 v242, v242, v250\n\t"
    "v_mfma_f32_16x16x32_bf16 v[24:27], a[240:243], a[104:107], v[24:27]\n\t"
    "buffer_load_dwordx4 a[236:239], v1, %[sW], s40 offen offset:1024 sc1\n\t"
    "v_cvt_pk_bf16_f32 v243, v243, v251\n\t"
    "v_mfma_f32_16x16x32_bf16 v[40:43], a[244:247], a[104:107], v[40:43]\n\t"
    "ds_write_b128 v2, v[240:243] offset:10240\n\t"
    "v_add_f32_e32 v244, v92, v232\n\t"
    "s_waitcnt lgkmcnt(15)\n\t"
    "v_mfma_f32_16x16x32_bf16 v[44:47], a[244:247], a[108:111], v[44:47]\n\t"
    "v_add_f32_e32 v245, v93, v233\n\t"
    "v_add_f32_e32 v246, v94, v234\n\t"
    "v_mfma_f32_16x16x32_bf16 v[28:31], a[240:243], a[108:111], v[28:31]\n\t"
    "v_add_f32_e32 v247, v95, v235\n\t"
    "v_add_f32_e32 v248, v108, v236\n\t"
    "s_waitcnt vmcnt(8) lgkmcnt(14)\n\t"
    "v_mfma_f32_16x16x32_bf16 v[16:19], a[248:251], a[112:115], v[16:19]\n\t"
    "buffer_load_dwordx4 a[240:243], v1, %[sW], s40 offen offset:2048 sc1\n\t"
    "v_add_f32_e32 v249, v109, v237\n\t"
    "v_mfma_f32_16x16x32_bf16 v[32:35], a[252:255], a[112:115], v[32:35]\n\t"
    "v_add_f32_e32 v250, v110, v238\n\t"
    "v_add_f32_e32 v251, v111, v239\n\t"
    "s_waitcnt lgkmcnt(13)\n\t"
    "v_mfma_f32_16x16x32_bf16 v[36:39], a[252:255], a[116:119], v[36:39]\n\t"
    "v_mul_f32_e32 v244, v244, v204\n\t"
    "v_mul_f32_e32 v245, v245, v205\n\t"
    "v_mfma_f32_16x16x32_bf16 v[20:23], a[248:251], a[116:119], v[20:23]\n\t"
    "v_mul_f32_e32 v246, v246, v206\n\t"
    "v_mul_f32_e32 v247, v247, v207\n\t"
    "s_waitcnt lgkmcnt(12)\n\t"
    "v_mfma_f32_16x16x32_bf16 v[24:27], a[248:251], a[120:123], v[24:27]\n\t"
    "buffer_load_dwordx4 a[244:247], v1, %[sW], s40 offen offset:3072 sc1\n\t"
    "s_add_u32 s40, s40, 4096\n\t"
    "v_mfma_f32_16x16x32_bf16 v[40:43], a[252:255], a[120:123], v[40:43]\n\t"
    "v_mul_f32_e32 v248, v248, v220\n\t"
    "v_mul_f32_e32 v249, v249, v221\n\t"
    "s_waitcnt lgkmcnt(11)\n\t"
    "v_mfma_f32_16x16x32_bf16 v[44:47], a[252:255], a[124:127], v[44:47]\n\t"
    "v_mul_f32_e32 v250, v250, v222\n\t"
    "v_mul_f32_e32 v251, v251, v223\n\t"
    "v_mfma_f32_16x16x32_bf16 v[28:31], a[248:251], a[124:127], v[28:31]\n\t"
    "v_cvt_pk_bf16_f32 v244, v244, v248\n\t"
    "v_cvt_pk_bf16_f32 v245, v245, v249\n\t"
    "s_waitcnt vmcnt(8) lgkmcnt(10)\n\t"
    "v_mfma_f32_16x16x32_bf16 v[16:19], v[252:255], a[144:147], v[16:19]\n\t"
    "buffer_load_dwordx4 a[248:251], v1, %[sW], s40 offen offset:0 sc1\n\t"
    "v_cvt_pk_bf16_f32 v246, v246, v250\n\t"
    "v_mfma_f32_16x16x32_bf16 v[32:35], a[208:211], a[144:147], v[32:35]\n\t"
    "v_cvt_pk_bf16_f32 v247, v247, v251\n\t"
    "ds_write_b128 v2, v[244:247] offset:11264\n\t"
    "s_waitcnt lgkmcnt(10)\n\t"
    "v_mfma_f32_16x16x32_bf16 v[36:39], a[208:211], a[148:151], v[36:39]\n\t"
    "ds_read_b128 v[232:235], v10 offset:0\n\t"
    "ds_read_b128 v[236:239], v10 offset:64\n\t"
    "v_mfma_f32_16x16x32_bf16 v[20:23], v[252:255], a[148:151], v[20:23]\n\t"
    "s_waitcnt lgkmcnt(0)\n\t"
    "s_barrier\n\t"
    "v_mfma_f32_16x16x32_bf16 v[24:27], v[252:255], a[152:155], v[24:27]\n\t"
    "buffer_load_dwordx4 a[252:255], v1, %[sW], s40 offen offset:1024 sc1\n\t"
    "ds_read_b128 a[192:195], v6 offset:49152\n\t"
    "v_mfma_f32_16x16x32_bf16 v[40:43], a[208:211], a[152:155], v[40:43]\n\t"
    "ds_read_b128 a[196:199], v6 offset:50176\n\t"
    "ds_read_b128 a[200:203], v6 offset:51200\n\t"
    "v_mfma_f32_16x16x32_bf16 v[44:47], a[208:211], a[156:159], v[44:47]\n\t"
    "ds_read_b128 a[204:207], v6 offset:52224\n\t"
    "ds_read_b128 a[32:35], v2 offset:8192\n\t"
    "v_mfma_f32_16x16x32_bf16 v[28:31], v[252:255], a[156:159], v[28:31]\n\t"
    "ds_read_b128 a[36:39], v2 offset:9216\n\t"
    "ds_read_b128 a[40:43], v2 offset:10240\n\t"
    "s_waitcnt vmcnt(8)\n\t"
    "v_mfma_f32_16x16x32_bf16 v[16:19], a[216:219], a[160:163], v[16:19]\n\t"
    "buffer_load_dwordx4 v[252:255], v1, %[sW], s40 offen offset:2048 sc1\n\t"
    "ds_read_b128 a[44:47], v2 offset:11264\n\t"
    "v_mfma_f32_16x16x32_bf16 v[32:35], a[220:223], a[160:163], v[32:35]\n\t"
    "ds_read_b128 a[80:83], v3 offset:8192\n\t"
    "ds_read_b128 a[84:87], v3 offset:9216\n\t"
    "v_mfma_f32_16x16x32_bf16 v[36:39], a[220:223], a[164:167], v[36:39]\n\t"
    "ds_read_b128 a[88:91], v3 offset:10240\n\t"
    "ds_read_b128 a[92:95], v3 offset:11264\n\t"
    "v_mfma_f32_16x16x32_bf16 v[20:23], a[216:219], a[164:167], v[20:23]\n\t"
    "ds_read_b128 a[128:131], v4 offset:8192\n\t"
    "ds_read_b128 a[132:135], v4 offset:9216\n\t"
    "v_mfma_f32_16x16x32_bf16 v[24:27], a[216:219], a[168:171], v[24:27]\n\t"
    "buffer_load_dwordx4 a[208:211], v1, %[sW], s40 offen offset:3072 sc1\n\t"
    "s_add_u32 s40, s40, 4096\n\t"
    "v_mfma_f32_16x16x32_bf16 v[40:43], a[220:223], a[168:171], v[40:43]\n\t"
    "ds_read_b128 a[136:139], v4 offset:10240\n\t"
    "ds_read_b128 a[140:143], v4 offset:11264\n\t"
    "v_mfma_f32_16x16x32_bf16 v[44:47], a[220:223], a[172:175], v[44:47]\n\t"
    "buffer_load_dwordx4 a[212:215], v1, %[sW], s40 offen offset:0 sc1\n\t"
    "ds_read_b128 a[176:179], v5 offset:8192\n\t"
    "v_mfma_f32_16x16x32_bf16 v[28:31], a[216:219], a[172:175], v[28:31]\n\t"
    "ds_read_b128 a[180:183], v5 offset:9216\n\t"
    "ds_read_b128 a[184:187], v5 offset:10240\n\t"
    "s_waitcnt vmcnt(10)\n\t"
    "v_mfma_f32_16x16x32_bf16 v[112:115], a[224:227], a[0:3], v[112:115]\n\t"
    "buffer_load_dwordx4 a[216:219], v1, %[sW], s40 offen offset:1024 sc1\n\t"
    "ds_read_b128 a[188:191], v5 offset:11264\n\t"
    "v_mfma_f32_16x16x32_bf16 v[116:119], a[224:227], a[4:7], v[116:119]\n\t"
    "v_mbcnt_lo_u32_b32 v11, -1, 0\n\t"
    "v_mbcnt_hi_u32_b32 v11, -1, v11\n\t"
    "v_mfma_f32_16x16x32_bf16 v[120:123], a[224:227], a[8:11], v[120:123]\n\t"
    "s_mul_i32 s42, %[wv], 12288\n\t"
    "s_add_u32 s42, s42, %[lds]\n\t"
    "v_mfma_f32_16x16x32_bf16 v[124:127], a[224:227], a[12:15], v[124:127]\n\t"
    "s_add_u32 s42, s42, 82176\n\t"
    "v_lshrrev_b32_e32 v12, 4, v11\n\t"
    "s_waitcnt vmcnt(10)\n\t"
    "v_mfma_f32_16x16x32_bf16 v[112:115], a[228:231], a[16:19], v[112:115]\n\t"
    "buffer_load_dwordx4 a[224:227], v1, %[sW], s40 offen offset:2048 sc1\n\t"
    "v_mul_u32_u24_e32 v12, 3, v12\n\t"
    "v_mfma_f32_16x16x32_bf16 v[116:119], a[228:231], a[20:23], v[116:119]\n\t"
    "v_and_b32_e32 v13, 15, v11\n\t"
    "v_lshlrev_b32_e32 v13, 4, v13\n\t"
    "v_mfma_f32_16x16x32_bf16 v[120:123], a[228:231], a[24:27], v[120:123]\n\t"
    "v_and_b32_e32 v14, 1, v12\n\t"
    "v_lshlrev_b32_e32 v14, 4, v14\n\t"
    "v_mfma_f32_16x16x32_bf16 v[124:127], a[228:231], a[28:31], v[124:127]\n\t"
    "v_and_b32_e32 v15, 2, v12\n\t"
    "v_lshl_or_b32 v14, v15, 5, v14\n\t"
    "s_waitcnt vmcnt(10)\n\t"
    "v_mfma_f32_16x16x32_bf16 v[112:115], a[232:235], a[96:99], v[112:115]\n\t"
    "buffer_load_dwordx4 a[228:231], v1, %[sW], s40 offen offset:3072 sc1\n\t"
    "s_add_u32 s40, s40, 4096\n\t"
    "v_mfma_f32_16x16x32_bf16 v[116:119], a[232:235], a[100:103], v[116:119]\n\t"
    "v_xor_b32_e32 v14, v14, v13\n\t"
    "v_and_b32_e32 v15, 3, v12\n\t"
    "v_mfma_f32_16x16x32_bf16 v[120:123], a[232:235], a[104:107], v[120:123]\n\t"
    "v_lshl_add_u32 v14, v15, 8, v14\n\t"
    "v_lshrrev_b32_e32 v15, 2, v12\n\t"
    "v_mfma_f32_16x16x32_bf16 v[124:127], a[232:235], a[108:111], v[124:127]\n\t"
    "v_lshl_add_u32 v14, v15, 12, v14\n\t"
    "v_add_u32_e32 v0, s42, v14\n\t"
    "s_waitcnt vmcnt(10)\n\t"
    "v_mfma_f32_16x16x32_bf16 v[112:115], a[236:239], a[112:115], v[112:115]\n\t"
    "buffer_load_dwordx4 a[232:235], v1, %[sW], s40 offen offset:0 sc1\n\t"
    "v_add_u32_e32 v12, 1, v12\n\t"
    "v_mfma_f32_16x16x32_bf16 v[116:119], a[236:239], a[116:119], v[116:119]\n\t"
    "v_and_b32_e32 v14, 1, v12\n\t"
    "v_lshlrev_b32_e32 v14, 4, v14\n\t"
    "v_mfma_f32_16x16x32_bf16 v[120:123], a[236:239], a[120:123], v[120:123]\n\t"
    "v_and_b32_e32 v15, 2, v12\n\t"
    "v_lshl_or_b32 v14, v15, 5, v14\n\t"
    "v_mfma_f32_16x16x32_bf16 v[124:127], a[236:239], a[124:127], v[124:127]\n\t"
    "v_xor_b32_e32 v14, v14, v13\n\t"
    "v_and_b32_e32 v15, 3, v12\n\t"
    "s_waitcnt vmcnt(9) lgkmcnt(15)\n\t"
    "v_mfma_f32_16x16x32_bf16 v[16:19], a[240:243], a[192:195], v[16:19]\n\t"
    "buffer_load_dwordx4 a[236:239], v1, %[sW], s40 offen offset:1024 sc1\n\t"
    "v_lshl_add_u32 v14, v15, 8, v14\n\t"
    "v_mfma_f32_16x16x32_bf16 v[32:35], a[244:247], a[192:195], v[32:35]\n\t"
    "v_lshrrev_b32_e32 v15, 2, v12\n\t"
    "v_lshl_add_u32 v14, v15, 12, v14\n\t"
    "v_mfma_f32_16x16x32_bf16 v[36:39], a[244:247], a[196:199], v[36:39]\n\t"
    "v_add_u32_e32 v3, s42, v14\n\t"
    "v_add_u32_e32 v12, 1, v12\n\t"
    "v_mfma_f32_16x16x32_bf16 v[20:23], a[240:243], a[196:199], v[20:23]\n\t"
    "v_and_b32_e32 v14, 1, v12\n\t"
    "v_lshlrev_b32_e32 v14, 4, v14\n\t"
    "v_mfma_f32_16x16x32_bf16 v[24:27], a[240:243], a[200:203], v[24:27]\n\t"
    "v_and_b32_e32 v15, 2, v12\n\t"
    "v_lshl_or_b32 v14, v15, 5, v14\n\t"
    "v_mfma_f32_16x16x32_bf16 v[40:43], a[244:247], a[200:203], v[40:43]\n\t"
    "v_xor_b32_e32 v14, v14, v13\n\t"
    "v_and_b32_e32 v15, 3, v12\n\t"
    "v_mfma_f32_16x16x32_bf16 v[44:47], a[244:247], a[204:207], v[44:47]\n\t"
    "v_lshl_add_u32 v14, v15, 8, v14\n\t"
    "v_lshrrev_b32_e32 v15, 2, v12\n\t"
    "v_mfma_f32_16x16x32_bf16 v[28:31], a[240:243], a[204:207], v[28:31]\n\t"
    "v_lshl_add_u32 v14, v15, 12, v14\n\t"
    "v_add_u32_e32 v4, s42, v14\n\t"
    "s_waitcnt vmcnt(8)\n\t"
    "v_mfma_f32_16x16x32_bf16 v[16:19], a[248:251], a[32:35], v[16:19]\n\t"
    "buffer_load_dwordx4 a[240:243], v1, %[sW], s40 offen offset:2048 sc1\n\t"
    "v_add_u32_e32 v9, 82176, v9\n\t"
    "v_mfma_f32_16x16x32_bf16 v[32:35], a[252:255], a[32:35], v[32:35]\n\t"
    "s_waitcnt lgkmcnt(14)\n\t"
    "v_mfma_f32_16x16x32_bf16 v[36:39], a[252:255], a[36:39], v[36:39]\n\t"
    "v_mfma_f32_16x16x32_bf16 v[20:23], a[248:251], a[36:39], v[20:23]\n\t"
    "s_waitcnt lgkmcnt(13)\n\t"
    "v_mfma_f32_16x16x32_bf16 v[24:27], a[248:251], a[40:43], v[24:27]\n\t"
    "buffer_load_dwordx4 a[244:247], v1, %[sW], s40 offen offset:3072 sc1\n\t"
    "s_add_u32 s40, s40, 4096\n\t"
    "v_mfma_f32_16x16x32_bf16 v[40:43], a[252:255], a[40:43], v[40:43]\n\t"
    "s_waitcnt lgkmcnt(12)\n\t"
    "v_mfma_f32_16x16x32_bf16 v[44:47], a[252:255], a[44:47], v[44:47]\n\t"
    "v_mfma_f32_16x16x32_bf16 v[28:31], a[248:251], a[44:47], v[28:31]\n\t"
    "s_waitcnt vmcnt(8) lgkmcnt(11)\n\t"
    "v_mfma_f32_16x16x32_bf16 v[16:19], v[252:255], a[80:83], v[16:19]\n\t"
    "buffer_load_dwordx4 a[248:251], v1, %[sW], s40 offen offset:0 sc1\n\t"
    "v_mfma_f32_16x16x32_bf16 v[32:35], a[208:211], a[80:83], v[32:35]\n\t"
    "s_waitcnt lgkmcnt(10)\n\t"
    "v_mfma_f32_16x16x32_bf16 v[36:39], a[208:211], a[84:87], v[36:39]\n\t"
    "v_mfma_f32_16x16x32_bf16 v[20:23], v[252:255], a[84:87], v[20:23]\n\t"
    "s_waitcnt lgkmcnt(9)\n\t"
    "v_mfma_f32_16x16x32_bf16 v[24:27], v[252:255], a[88:91], v[24:27]\n\t"
    "buffer_load_dwordx4 a[252:255], v1, %[sW], s40 offen offset:1024 sc1\n\t"
    "v_mfma_f32_16x16x32_bf16 v[40:43], a[208:211], a[88:91], v[40:43]\n\t"
    "s_waitcnt lgkmcnt(8)\n\t"
    "v_mfma_f32_16x16x32_bf16 v[44:47], a[208:211], a[92:95], v[44:47]\n\t"
    "v_mfma_f32_16x16x32_bf16 v[28:31], v[252:255], a[92:95], v[28:31]\n\t"
    "s_waitcnt vmcnt(8) lgkmcnt(7)\n\t"
    "v_mfma_f32_16x16x32_bf16 v[16:19], a[212:215], a[128:131], v[16:19]\n\t"
    "buffer_load_dwordx4 v[252:255], v1, %[sW], s40 offen offset:2048 sc1\n\t"
    "v_mfma_f32_16x16x32_bf16 v[32:35], a[216:219], a[128:131], v[32:35]\n\t"
    "s_waitcnt lgkmcnt(6)\n\t"
    "v_mfma_f32_16x16x32_bf16 v[36:39], a[216:219], a[132:135], v[36:39]\n\t"
    "v_mfma_f32_16x16x32_bf16 v[20:23], a[212:215], a[132:135], v[20:23]\n\t"
    "s_waitcnt lgkmcnt(5)\n\t"
    "v_mfma_f32_16x16x32_bf16 v[24:27], a[212:215], a[136:139], v[24:27]\n\t"
    "buffer_load_dwordx4 a[208:211], v1, %[sW], s40 offen offset:3072 sc1\n\t"
    "s_add_u32 s40, s40, 4096\n\t"
    "v_mfma_f32_16x16x32_bf16 v[40:43], a[216:219], a[136:139], v[40:43]\n\t"
    "s_waitcnt lgkmcnt(4)\n\t"
    "v_mfma_f32_16x16x32_bf16 v[44:47], a[216:219], a[140:143], v[44:47]\n\t"
    "v_mfma_f32_16x16x32_bf16 v[28:31], a[212:215], a[140:143], v[28:31]\n\t"
    "s_waitcnt vmcnt(8) lgkmcnt(3)\n\t"
    "v_mfma_f32_16x16x32_bf16 v[16:19], a[224:227], a[176:179], v[16:19]\n\t"
    "buffer_load_dwordx4 a[212:215], v1, %[sW], s40 offen offset:0 sc1\n\t"
    "v_mfma_f32_16x16x32_bf16 v[32:35], a[228:231], a[176:179], v[32:35]\n\t"
    "s_waitcnt lgkmcnt(2)\n\t"
    "v_mfma_f32_16x16x32_bf16 v[36:39], a[228:231], a[180:183], v[36:39]\n\t"
    "v_mfma_f32_16x16x32_bf16 v[20:23], a[224:227], a[180:183], v[20:23]\n\t"
    "s_waitcnt lgkmcnt(1)\n\t"
    "v_mfma_f32_16x16x32_bf16 v[24:27], a[224:227], a[184:187], v[24:27]\n\t"
    "buffer_load_dwordx4 a[216:219], v1, %[sW], s40 offen offset:1024 sc1\n\t"
    "v_mfma_f32_16x16x32_bf16 v[40:43], a[228:231], a[184:187], v[40:43]\n\t"
    "s_waitcnt lgkmcnt(0)\n\t"
    "v_mfma_f32_16x16x32_bf16 v[44:47], a[228:231], a[188:191], v[44:47]\n\t"
    "buffer_load_dwordx4 a[220:223], v1, %[sW], s40 offen offset:2048 sc1\n\t"
    "v_mfma_f32_16x16x32_bf16 v[28:31], a[224:227], a[188:191], v[28:31]\n\t"
    "s_waitcnt vmcnt(10)\n\t"
    "v_mfma_f32_16x16x32_bf16 v[112:115], a[232:235], a[192:195], v[112:115]\n\t"
    "buffer_load_dwordx4 a[224:227], v1, %[sW], s40 offen offset:3072 sc1\n\t"
    "s_add_u32 s40, s40, 4096\n\t"
    "v_mfma_f32_16x16x32_bf16 v[116:119], a[232:235], a[196:199], v[116:119]\n\t"
    "v_mfma_f32_16x16x32_bf16 v[120:123], a[232:235], a[200:203], v[120:123]\n\t"
    "v_add_f32_e32 v240, v16, v232\n\t"
    "v_add_f32_e32 v241, v17, v233\n\t"
    "v_mfma_f32_16x16x32_bf16 v[124:127], a[232:235], a[204:207], v[124:127]\n\t"
    "v_add_f32_e32 v242, v18, v234\n\t"
    "v_add_f32_e32 v243, v19, v235\n\t"
    "s_waitcnt vmcnt(10)\n\t"
    "v_mfma_f32_16x16x32_bf16 v[112:115], a[236:239], a[32:35], v[112:115]\n\t"
    "buffer_load_dwordx4 a[232:235], v1, %[sW], s40 offen offset:0 sc1\n\t"
    "v_add_f32_e32 v248, v32, v236\n\t"
    "v_mfma_f32_16x16x32_bf16 v[116:119], a[236:239], a[36:39], v[116:119]\n\t"
    "v_add_f32_e32 v249, v33, v237\n\t"
    "v_add_f32_e32 v250, v34, v238\n\t"
    "v_mfma_f32_16x16x32_bf16 v[120:123], a[236:239], a[40:43], v[120:123]\n\t"
    "v_add_f32_e32 v251, v35, v239\n\t"
    "v_mul_f32_e32 v240, v240, v128\n\t"
    "v_mfma_f32_16x16x32_bf16 v[124:127], a[236:239], a[44:47], v[124:127]\n\t"
    "v_mul_f32_e32 v241, v241, v129\n\t"
    "v_mul_f32_e32 v242, v242, v130\n\t"
    "s_waitcnt vmcnt(10)\n\t"
    "v_mfma_f32_16x16x32_bf16 v[112:115], a[240:243], a[128:131], v[112:115]\n\t"
    "buffer_load_dwordx4 a[236:239], v1, %[sW], s40 offen offset:1024 sc1\n\t"
    "v_mul_f32_e32 v243, v243, v131\n\t"
    "v_mfma_f32_16x16x32_bf16 v[116:119], a[240:243], a[132:135], v[116:119]\n\t"
    "v_mul_f32_e32 v248, v248, v144\n\t"
    "v_mul_f32_e32 v249, v249, v145\n\t"
    "v_mfma_f32_16x16x32_bf16 v[120:123], a[240:243], a[136:139], v[120:123]\n\t"
    "v_mul_f32_e32 v250, v250, v146\n\t"
    "v_mul_f32_e32 v251, v251, v147\n\t"
    "v_mfma_f32_16x16x32_bf16 v[124:127], a[240:243], a[140:143], v[124:127]\n\t"
    "v_cvt_pk_bf16_f32 v240, v240, v248\n\t"
    "v_cvt_pk_bf16_f32 v241, v241, v249\n\t"
    "s_waitcnt vmcnt(9)\n\t"
    "v_mfma_f32_16x16x32_bf16 v[48:51], a[244:247], a[0:3], v[48:51]\n\t"
    "buffer_load_dwordx4 a[240:243], v1, %[sW], s40 offen offset:2048 sc1\n\t"
    "v_cvt_pk_bf16_f32 v242, v242, v250\n\t"
    "v_mfma_f32_16x16x32_bf16 v[64:67], a[248:251], a[0:3], v[64:67]\n\t"
    "v_cvt_pk_bf16_f32 v243, v243, v251\n\t"
    "ds_write_b32 v0, v240 offset:0\n\t"
    "v_mfma_f32_16x16x32_bf16 v[68:71], a[248:251], a[4:7], v[68:71]\n\t"
    "ds_write_b32 v0, v241 offset:12\n\t"
    "ds_write_b32 v3, v242 offset:8\n\t"
    "v_mfma_f32_16x16x32_bf16 v[52:55], a[244:247], a[4:7], v[52:55]\n\t"
    "ds_write_b32 v4, v243 offset:4\n\t"
    "v_add_f32_e32 v244, v20, v232\n\t"
    "v_mfma_f32_16x16x32_bf16 v[56:59], a[244:247], a[8:11], v[56:59]\n\t"
    "v_add_f32_e32 v245, v21, v233\n\t"
    "v_add_f32_e32 v246, v22, v234\n\t"
    "v_mfma_f32_16x16x32_bf16 v[72:75], a[248:251], a[8:11], v[72:75]\n\t"
    "v_add_f32_e32 v247, v23, v235\n\t"
    "v_add_f32_e32 v248, v36, v236\n\t"
    "v_mfma_f32_16x16x32_bf16 v[76:79], a[248:251], a[12:15], v[76:79]\n\t"
    "v_add_f32_e32 v249, v37, v237\n\t"
    "v_add_f32_e32 v250, v38, v238\n\t"
    "v_mfma_f32_16x16x32_bf16 v[60:63], a[244:247], a[12:15], v[60:63]\n\t"
    "v_add_f32_e32 v251, v39, v239\n\t"
    "v_mul_f32_e32 v244, v244, v132\n\t"
    "s_waitcnt vmcnt(8)\n\t"
    "v_mfma_f32_16x16x32_bf16 v[48:51], a[252:255], a[16:19], v[48:51]\n\t"
    "buffer_load_dwordx4 a[244:247], v1, %[sW], s40 offen offset:3072 sc1\n\t"
    "s_add_u32 s40, s40, 4096\n\t"
    "v_mfma_f32_16x16x32_bf16 v[64:67], v[252:255], a[16:19], v[64:67]\n\t"
    "v_mul_f32_e32 v245, v245, v133\n\t"
    "v_mul_f32_e32 v246, v246, v134\n\t"
    "v_mfma_f32_16x16x32_bf16 v[68:71], v[252:255], a[20:23], v[68:71]\n\t"
    "v_mul_f32_e32 v247, v247, v135\n\t"
    "v_mul_f32_e32 v248, v248, v148\n\t"
    "v_mfma_f32_16x16x32_bf16 v[52:55], a[252:255], a[20:23], v[52:55]\n\t"
    "v_mul_f32_e32 v249, v249, v149\n\t"
    "v_mul_f32_e32 v250, v250, v150\n\t"
    "v_mfma_f32_16x16x32_bf16 v[56:59], a[252:255], a[24:27], v[56:59]\n\t"
    "buffer_load_dwordx4 a[248:251], v1, %[sW], s40 offen offset:0 sc1\n\t"
    "v_mul_f32_e32 v251, v251, v151\n\t"
    "v_mfma_f32_16x16x32_bf16 v[72:75], v[252:255], a[24:27], v[72:75]\n\t"
    "v_cvt_pk_bf16_f32 v244, v244, v248\n\t"
    "v_cvt_pk_bf16_f32 v245, v245, v249\n\t"
    "v_mfma_f32_16x16x32_bf16 v[76:79], v[252:255], a[28:31], v[76:79]\n\t"
    "v_cvt_pk_bf16_f32 v246, v246, v250\n\t"
    "v_cvt_pk_bf16_f32 v247, v247, v251\n\t"
    "v_mfma_f32_16x16x32_bf16 v[60:63], a[252:255], a[28:31], v[60:63]\n\t"
    "ds_write_b32 v0, v244 offset:1024\n\t"
    "ds_write_b32 v0, v245 offset:1036\n\t"
    "s_waitcnt vmcnt(8)\n\t"
    "v_mfma_f32_16x16x32_bf16 v[48:51], a[208:211], a[48:51], v[48:51]\n\t"
    "buffer_load_dwordx4 a[252:255], v1, %[sW], s40 offen offset:1024 sc1\n\t"
    "ds_write_b32 v3, v246 offset:1032\n\t"
    "v_mfma_f32_16x16x32_bf16 v[64:67], a[212:215], a[48:51], v[64:67]\n\t"
    "ds_write_b32 v4, v247 offset:1028\n\t"
    "v_add_f32_e32 v240, v24, v232\n\t"
    "v_mfma_f32_16x16x32_bf16 v[68:71], a[212:215], a[52:55], v[68:71]\n\t"
    "v_add_f32_e32 v241, v25, v233\n\t"
    "v_add_f32_e32 v242, v26, v234\n\t"
    "v_mfma_f32_16x16x32_bf16 v[52:55], a[208:211], a[52:55], v[52:55]\n\t"
    "v_add_f32_e32 v243, v27, v235\n\t"
    "v_add_f32_e32 v248, v40, v236\n\t"
    "v_mfma_f32_16x16x32_bf16 v[56:59], a[208:211], a[56:59], v[56:59]\n\t"
    "buffer_load_dwordx4 v[252:255], v1, %[sW], s40 offen offset:2048 sc1\n\t"
    "v_add_f32_e32 v249, v41, v237\n\t"
    "v_mfma_f32_16x16x32_bf16 v[72:75], a[212:215], a[56:59], v[72:75]\n\t"
    "v_add_f32_e32 v250, v42, v238\n\t"
    "v_add_f32_e32 v251, v43, v239\n\t"
    "v_mfma_f32_16x16x32_bf16 v[76:79], a[212:215], a[60:63], v[76:79]\n\t"
    "v_mul_f32_e32 v240, v240, v136\n\t"
    "v_mul_f32_e32 v241, v241, v137\n\t"
    "v_mfma_f32_16x16x32_bf16 v[60:63], a[208:211], a[60:63], v[60:63]\n\t"
    "v_mul_f32_e32 v242, v242, v138\n\t"
    "v_mul_f32_e32 v243, v243, v139\n\t"
    "s_waitcnt vmcnt(8)\n\t"
    "v_mfma_f32_16x16x32_bf16 v[48:51], a[216:219], a[64:67], v[48:51]\n\t"
    "buffer_load_dwordx4 a[208:211], v1, %[sW], s40 offen offset:3072 sc1\n\t"
    "s_add_u32 s40, s40, 4096\n\t"
    "v_mfma_f32_16x16x32_bf16 v[64:67], a[220:223], a[64:67], v[64:67]\n\t"
    "v_mul_f32_e32 v248, v248, v152\n\t"
    "v_mul_f32_e32 v249, v249, v153\n\t"
    "v_mfma_f32_16x16x32_bf16 v[68:71], a[220:223], a[68:71], v[68:71]\n\t"
    "v_mul_f32_e32 v250, v250, v154\n\t"
    "v_mul_f32_e32 v251, v251, v155\n\t"
    "v_mfma_f32_16x16x32_bf16 v[52:55], a[216:219], a[68:71], v[52:55]\n\t"
    "v_cvt_pk_bf16_f32 v240, v240, v248\n\t"
    "v_cvt_pk_bf16_f32 v241, v241, v249\n\t"
    "v_mfma_f32_16x16x32_bf16 v[56:59], a[216:219], a[72:75], v[56:59]\n\t"
    "buffer_load_dwordx4 a[212:215], v1, %[sW], s40 offen offset:0 sc1\n\t"
    "v_cvt_pk_bf16_f32 v242, v242, v250\n\t"
    "v_mfma_f32_16x16x32_bf16 v[72:75], a[220:223], a[72:75], v[72:75]\n\t"
    "v_cvt_pk_bf16_f32 v243, v243, v251\n\t"
    "ds_write_b32 v0, v240 offset:2048\n\t"
    "v_mfma_f32_16x16x32_bf16 v[76:79], a[220:223], a[76:79], v[76:79]\n\t"
    "ds_write_b32 v0, v241 offset:2060\n\t"
    "ds_write_b32 v3, v242 offset:2056\n\t"
    "v_mfma_f32_16x16x32_bf16 v[60:63], a[216:219], a[76:79], v[60:63]\n\t"
    "ds_write_b32 v4, v243 offset:2052\n\t"
    "v_add_f32_e32 v244, v28, v232\n\t"
    "s_waitcnt vmcnt(8)\n\t"
    "v_mfma_f32_16x16x32_bf16 v[48:51], a[224:227], a[96:99], v[48:51]\n\t"
    "buffer_load_dwordx4 a[216:219], v1, %[sW], s40 offen offset:1024 sc1\n\t"
    "v_add_f32_e32 v245, v29, v233\n\t"
    "v_mfma_f32_16x16x32_bf16 v[64:67], a[232:235], a[96:99], v[64:67]\n\t"
    "v_add_f32_e32 v246, v30, v234\n\t"
    "v_add_f32_e32 v247, v31, v235\n\t"
    "v_mfma_f32_16x16x32_bf16 v[68:71], a[232:235], a[100:103], v[68:71]\n\t"
    "v_add_f32_e32 v248, v44, v236\n\t"
    "v_add_f32_e32 v249, v45, v237\n\t"
    "v_mfma_f32_16x16x32_bf16 v[52:55], a[224:227], a[100:103], v[52:55]\n\t"
    "v_add_f32_e32 v250, v46, v238\n\t"
    "v_add_f32_e32 v251, v47, v239\n\t"
    "v_mfma_f32_16x16x32_bf16 v[56:59], a[224:227], a[104:107], v[56:59]\n\t"
    "buffer_load_dwordx4 a[220:223], v1, %[sW], s40 offen offset:2048 sc1\n\t"
    "v_mul_f32_e32 v244, v244, v140\n\t"
    "v_mfma_f32_16x16x32_bf16 v[72:75], a[232:235], a[104:107], v[72:75]\n\t"
    "v_mul_f32_e32 v245, v245, v141\n\t"
    "v_mul_f32_e32 v246, v246, v142\n\t"
    "v_mfma_f32_16x16x32_bf16 v[76:79], a[232:235], a[108:111], v[76:79]\n\t"
    "v_mul_f32_e32 v247, v247, v143\n\t"
    "v_mul_f32_e32 v248, v248, v156\n\t"
    "v_mfma_f32_16x16x32_bf16 v[60:63], a[224:227], a[108:111], v[60:63]\n\t"
    "v_mul_f32_e32 v249, v249, v157\n\t"
    "v_mul_f32_e32 v250, v250, v158\n\t"
    "s_waitcnt vmcnt(8)\n\t"
    "v_mfma_f32_16x16x32_bf16 v[48:51], a[236:239], a[112:115], v[48:51]\n\t"
    "buffer_load_dwordx4 a[224:227], v1, %[sW], s40 offen offset:3072 sc1\n\t"
    "s_add_u32 s40, s40, 4096\n\t"
    "v_mfma_f32_16x16x32_bf16 v[64:67], a[240:243], a[112:115], v[64:67]\n\t"
    "v_mul_f32_e32 v251, v251, v159\n\t"
    "v_cvt_pk_bf16_f32 v244, v244, v248\n\t"
    "v_mfma_f32_16x16x32_bf16 v[68:71], a[240:243], a[116:119], v[68:71]\n\t"
    "v_cvt_pk_bf16_f32 v245, v245, v249\n\t"
    "v_cvt_pk_bf16_f32 v246, v246, v250\n\t"
    "v_mfma_f32_16x16x32_bf16 v[52:55], a[236:239], a[116:119], v[52:55]\n\t"
    "v_cvt_pk_bf16_f32 v247, v247, v251\n\t"
    "ds_write_b32 v0, v244 offset:3072\n\t"
    "v_mfma_f32_16x16x32_bf16 v[56:59], a[236:239], a[120:123], v[56:59]\n\t"
    "buffer_load_dwordx4 a[228:231], v1, %[sW], s40 offen offset:0 sc1\n\t"
    "ds_write_b32 v0, v245 offset:3084\n\t"
    "v_mfma_f32_16x16x32_bf16 v[72:75], a[240:243], a[120:123], v[72:75]\n\t"
    "ds_write_b32 v3, v246 offset:3080\n\t"
    "ds_write_b32 v4, v247 offset:3076\n\t"
    "v_mfma_f32_16x16x32_bf16 v[76:79], a[240:243], a[124:127], v[76:79]\n\t"
    "buffer_load_dwordx4 a[232:235], v1, %[sW], s40 offen offset:1024 sc1\n\t"
    "ds_read_b128 v[232:235], v10 offset:128\n\t"
    "v_mfma_f32_16x16x32_bf16 v[60:63], a[236:239], a[124:127], v[60:63]\n\t"
    "ds_read_b128 v[236:239], v10 offset:192\n\t"
    "ds_write_b128 v7, v[112:115] offset:0\n\t"
    "s_waitcnt vmcnt(9)\n\t"
    "v_mfma_f32_16x16x32_bf16 v[48:51], a[244:247], a[144:147], v[48:51]\n\t"
    "buffer_load_dwordx4 a[240:243], v1, %[sW], s40 offen offset:2048 sc1\n\t"
    "ds_write_b128 v7, v[116:119] offset:1024\n\t"
    "v_mfma_f32_16x16x32_bf16 v[64:67], a[248:251], a[144:147], v[64:67]\n\t"
    "ds_write_b128 v7, v[120:123] offset:2048\n\t"
    "ds_write_b128 v7, v[124:127] offset:3072\n\t"
    "v_mfma_f32_16x16x32_bf16 v[68:71], a[248:251], a[148:151], v[68:71]\n\t"
    "v_mfma_f32_16x16x32_bf16 v[52:55], a[244:247], a[148:151], v[52:55]\n\t"
    "v_mfma_f32_16x16x32_bf16 v[56:59], a[244:247], a[152:155], v[56:59]\n\t"
    "v_mfma_f32_16x16x32_bf16 v[72:75], a[248:251], a[152:155], v[72:75]\n\t"
    "v_mfma_f32_16x16x32_bf16 v[76:79], a[248:251], a[156:159], v[76:79]\n\t"
    "v_mfma_f32_16x16x32_bf16 v[60:63], a[244:247], a[156:159], v[60:63]\n\t"
    "s_waitcnt vmcnt(8)\n\t"
    "v_mfma_f32_16x16x32_bf16 v[48:51], a[252:255], a[160:163], v[48:51]\n\t"
    "buffer_load_dwordx4 a[244:247], v1, %[sW], s40 offen offset:3072 sc1\n\t"
    "s_add_u32 s40, s40, 4096\n\t"
    "v_mfma_f32_16x16x32_bf16 v[64:67], v[252:255], a[160:163], v[64:67]\n\t"
    "v_mfma_f32_16x16x32_bf16 v[68:71], v[252:255], a[164:167], v[68:71]\n\t"
    "v_mfma_f32_16x16x32_bf16 v[52:55], a[252:255], a[164:167], v[52:55]\n\t"
    "v_mfma_f32_16x16x32_bf16 v[56:59], a[252:255], a[168:171], v[56:59]\n\t"
    "buffer_load_dwordx4 a[248:251], v1, %[sW], s40 offen offset:0 sc1\n\t"
    "v_mfma_f32_16x16x32_bf16 v[72:75], v[252:255], a[168:171], v[72:75]\n\t"
    "v_mfma_f32_16x16x32_bf16 v[76:79], v[252:255], a[172:175], v[76:79]\n\t"
    "v_mfma_f32_16x16x32_bf16 v[60:63], a[252:255], a[172:175], v[60:63]\n\t"
    "s_waitcnt vmcnt(8)\n\t"
    "v_mfma_f32_16x16x32_bf16 v[48:51], a[208:211], a[192:195], v[48:51]\n\t"
    "buffer_load_dwordx4 a[252:255], v1, %[sW], s40 offen offset:1024 sc1\n\t"
    "v_mfma_f32_16x16x32_bf16 v[64:67], a[212:215], a[192:195], v[64:67]\n\t"
    "v_mfma_f32_16x16x32_bf16 v[68:71], a[212:215], a[196:199], v[68:71]\n\t"
    "v_mfma_f32_16x16x32_bf16 v[52:55], a[208:211], a[196:199], v[52:55]\n\t"
    "v_mfma_f32_16x16x32_bf16 v[56:59], a[208:211], a[200:203], v[56:59]\n\t"
    "buffer_load_dwordx4 v[252:255], v1, %[sW], s40 offen offset:2048 sc1\n\t"
    "v_mfma_f32_16x16x32_bf16 v[72:75], a[212:215], a[200:203], v[72:75]\n\t"
    "v_mfma_f32_16x16x32_bf16 v[76:79], a[212:215], a[204:207], v[76:79]\n\t"
    "v_mfma_f32_16x16x32_bf16 v[60:63], a[208:211], a[204:207], v[60:63]\n\t"
    "s_waitcnt vmcnt(8)\n\t"
    "v_mfma_f32_16x16x32_bf16 v[48:51], a[216:219], a[32:35], v[48:51]\n\t"
    "buffer_load_dwordx4 a[208:211], v1, %[sW], s40 offen offset:3072 sc1\n\t"
    "s_add_u32 s40, s40, 4096\n\t"
    "v_mfma_f32_16x16x32_bf16 v[64:67], a[220:223], a[32:35], v[64:67]\n\t"
    "v_mfma_f32_16x16x32_bf16 v[68:71], a[220:223], a[36:39], v[68:71]\n\t"
    "v_mfma_f32_16x16x32_bf16 v[52:55], a[216:219], a[36:39], v[52:55]\n\t"
    "v_mfma_f32_16x16x32_bf16 v[56:59], a[216:219], a[40:43], v[56:59]\n\t"
    "buffer_load_dwordx4 a[212:215], v1, %[sW], s40 offen offset:0 sc1\n\t"
    "v_mfma_f32_16x16x32_bf16 v[72:75], a[220:223], a[40:43], v[72:75]\n\t"
    "v_mfma_f32_16x16x32_bf16 v[76:79], a[220:223], a[44:47], v[76:79]\n\t"
    "v_mfma_f32_16x16x32_bf16 v[60:63], a[216:219], a[44:47], v[60:63]\n\t"
    "s_waitcnt vmcnt(8)\n\t"
    "v_mfma_f32_16x16x32_bf16 v[48:51], a[224:227], a[80:83], v[48:51]\n\t"
    "buffer_load_dwordx4 a[216:219], v1, %[sW], s40 offen offset:1024 sc1\n\t"
    "v_mfma_f32_16x16x32_bf16 v[64:67], a[228:231], a[80:83], v[64:67]\n\t"
    "v_mfma_f32_16x16x32_bf16 v[68:71], a[228:231], a[84:87], v[68:71]\n\t"
    "v_mfma_f32_16x16x32_bf16 v[52:55], a[224:227], a[84:87], v[52:55]\n\t"
    "v_mfma_f32_16x16x32_bf16 v[56:59], a[224:227], a[88:91], v[56:59]\n\t"
    "buffer_load_dwordx4 a[220:223], v1, %[sW], s40 offen offset:2048 sc1\n\t"
    "v_mfma_f32_16x16x32_bf16 v[72:75], a[228:231], a[88:91], v[72:75]\n\t"
    "v_mfma_f32_16x16x32_bf16 v[76:79], a[228:231], a[92:95], v[76:79]\n\t"
    "v_mfma_f32_16x16x32_bf16 v[60:63], a[224:227], a[92:95], v[60:63]\n\t"
    "s_waitcnt vmcnt(8)\n\t"
    "v_mfma_f32_16x16x32_bf16 v[48:51], a[232:235], a[128:131], v[48:51]\n\t"
    "buffer_load_dwordx4 a[224:227], v1, %[sW], s40 offen offset:3072 sc1\n\t"
    "s_add_u32 s40, s40, 4096\n\t"
    "v_mfma_f32_16x16x32_bf16 v[64:67], a[240:243], a[128:131], v[64:67]\n\t"
    "v_mfma_f32_16x16x32_bf16 v[68:71], a[240:243], a[132:135], v[68:71]\n\t"
    "v_mfma_f32_16x16x32_bf16 v[52:55], a[232:235], a[132:135], v[52:55]\n\t"
    "v_mfma_f32_16x16x32_bf16 v[56:59], a[232:235], a[136:139], v[56:59]\n\t"
    "buffer_load_dwordx4 a[228:231], v1, %[sW], s40 offen offset:0 sc1\n\t"
    "v_mfma_f32_16x16x32_bf16 v[72:75], a[240:243], a[136:139], v[72:75]\n\t"
    "v_mfma_f32_16x16x32_bf16 v[76:79], a[240:243], a[140:143], v[76:79]\n\t"
    "v_mfma_f32_16x16x32_bf16 v[60:63], a[232:235], a[140:143], v[60:63]\n\t"
    "s_waitcnt vmcnt(8)\n\t"
    "v_mfma_f32_16x16x32_bf16 v[48:51], a[244:247], a[176:179], v[48:51]\n\t"
    "buffer_load_dwordx4 a[232:235], v1, %[sW], s40 offen offset:1024 sc1\n\t"
    "v_mfma_f32_16x16x32_bf16 v[64:67], a[248:251], a[176:179], v[64:67]\n\t"
    "v_mfma_f32_16x16x32_bf16 v[68:71], a[248:251], a[180:183], v[68:71]\n\t"
    "v_mfma_f32_16x16x32_bf16 v[52:55], a[244:247], a[180:183], v[52:55]\n\t"
    "v_mfma_f32_16x16x32_bf16 v[56:59], a[244:247], a[184:187], v[56:59]\n\t"
    "buffer_load_dwordx4 a[236:239], v1, %[sW], s40 offen offset:2048 sc1\n\t"
    "v_mfma_f32_16x16x32_bf16 v[72:75], a[248:251], a[184:187], v[72:75]\n\t"
    "v_mfma_f32_16x16x32_bf16 v[76:79], a[248:251], a[188:191], v[76:79]\n\t"
    "buffer_load_dwordx4 a[240:243], v1, %[sW], s40 offen offset:3072 sc1\n\t"
    "s_add_u32 s40, s40, 4096\n\t"
    "v_mfma_f32_16x16x32_bf16 v[60:63], a[244:247], a[188:191], v[60:63]\n\t"
    "s_waitcnt vmcnt(9)\n\t"
    "v_mfma_f32_16x16x32_bf16 v[80:83], a[252:255], a[0:3], v[80:83]\n\t"
    "buffer_load_dwordx4 a[248:251], v1, %[sW], s40 offen offset:0 sc1\n\t"
    "v_mfma_f32_16x16x32_bf16 v[96:99], v[252:255], a[0:3], v[96:99]\n\t"
    "v_mfma_f32_16x16x32_bf16 v[100:103], v[252:255], a[4:7], v[100:103]\n\t"
    "s_waitcnt lgkmcnt(5)\n\t"
    "v_add_f32_e32 v240, v48, v232\n\t"
    "v_add_f32_e32 v241, v49, v233\n\t"
    "v_mfma_f32_16x16x32_bf16 v[84:87], a[252:255], a[4:7], v[84:87]\n\t"
    "v_add_f32_e32 v242, v50, v234\n\t"
    "v_add_f32_e32 v243, v51, v235\n\t"
    "v_mfma_f32_16x16x32_bf16 v[88:91], a[252:255], a[8:11], v[88:91]\n\t"
    "s_waitcnt lgkmcnt(4)\n\t"
    "v_add_f32_e32 v248, v64, v236\n\t"
    "v_add_f32_e32 v249, v65, v237\n\t"
    "v_mfma_f32_16x16x32_bf16 v[104:107], v[252:255], a[8:11], v[104:107]\n\t"
    "v_add_f32_e32 v250, v66, v238\n\t"
    "v_add_f32_e32 v251, v67, v239\n\t"
    "v_mfma_f32_16x16x32_bf16 v[108:111], v[252:255], a[12:15], v[108:111]\n\t"
    "v_mul_f32_e32 v240, v240, v160\n\t"
    "v_mul_f32_e32 v241, v241, v161\n\t"
    "v_mfma_f32_16x16x32_bf16 v[92:95], a[252:255], a[12:15], v[92:95]\n\t"
    "v_mul_f32_e32 v242, v242, v162\n\t"
    "v_mul_f32_e32 v243, v243, v163\n\t"
    "s_waitcnt vmcnt(8)\n\t"
    "v_mfma_f32_16x16x32_bf16 v[80:83], a[208:211], a[16:19], v[80:83]\n\t"
    "buffer_load_dwordx4 a[252:255], v1, %[sW], s40 offen offset:1024 sc1\n\t"
    "v_mul_f32_e32 v248, v248, v176\n\t"
    "v_mfma_f32_16x16x32_bf16 v[96:99], a[212:215], a[16:19], v[96:99]\n\t"
    "v_mul_f32_e32 v249, v249, v177\n\t"
    "v_mul_f32_e32 v250, v250, v178\n\t"
    "v_mfma_f32_16x16x32_bf16 v[100:103], a[212:215], a[20:23], v[100:103]\n\t"
    "v_mul_f32_e32 v251, v251, v179\n\t"
    "v_cvt_pk_bf16_f32 v240, v240, v248\n\t"
    "v_mfma_f32_16x16x32_bf16 v[84:87], a[208:211], a[20:23], v[84:87]\n\t"
    "v_cvt_pk_bf16_f32 v241, v241, v249\n\t"
    "v_cvt_pk_bf16_f32 v242, v242, v250\n\t"
    "v_mfma_f32_16x16x32_bf16 v[88:91], a[208:211], a[24:27], v[88:91]\n\t"
    "buffer_load_dwordx4 v[252:255], v1, %[sW], s40 offen offset:2048 sc1\n\t"
    "v_cvt_pk_bf16_f32 v243, v243, v251\n\t"
    "v_mfma_f32_16x16x32_bf16 v[104:107], a[212:215], a[24:27], v[104:107]\n\t"
    "ds_write_b32 v0, v240 offset:4\n\t"
    "ds_write_b32 v3, v241 offset:0\n\t"
    "v_mfma_f32_16x16x32_bf16 v[108:111], a[212:215], a[28:31], v[108:111]\n\t"
    "ds_write_b32 v3, v242 offset:12\n\t"
    "ds_write_b32 v4, v243 offset:8\n\t"
    "v_mfma_f32_16x16x32_bf16 v[92:95], a[208:211], a[28:31], v[92:95]\n\t"
    "v_add_f32_e32 v244, v52, v232\n\t"
    "v_add_f32_e32 v245, v53, v233\n\t"
    "s_waitcnt vmcnt(8)\n\t"
    "v_mfma_f32_16x16x32_bf16 v[80:83], a[216:219], a[48:51], v[80:83]\n\t"
    "buffer_load_dwordx4 a[208:211], v1, %[sW], s40 offen offset:3072 sc1\n\t"
    "s_add_u32 s40, s40, 4096\n\t"
    "v_mfma_f32_16x16x32_bf16 v[96:99], a[220:223], a[48:51], v[96:99]\n\t"
    "v_add_f32_e32 v246, v54, v234\n\t"
    "v_add_f32_e32 v247, v55, v235\n\t"
    "v_mfma_f32_16x16x32_bf16 v[100:103], a[220:223], a[52:55], v[100:103]\n\t"
    "v_add_f32_e32 v248, v68, v236\n\t"
    "v_add_f32_e32 v249, v69, v237\n\t"
    "v_mfma_f32_16x16x32_bf16 v[84:87], a[216:219], a[52:55], v[84:87]\n\t"
    "v_add_f32_e32 v250, v70, v238\n\t"
    "v_add_f32_e32 v251, v71, v239\n\t"
    "v_mfma_f32_16x16x32_bf16 v[88:91], a[216:219], a[56:59], v[88:91]\n\t"
    "buffer_load_dwordx4 a[212:215], v1, %[sW], s40 offen offset:0 sc1\n\t"
    "v_mul_f32_e32 v244, v244, v164\n\t"
    "v_mfma_f32_16x16x32_bf16 v[104:107], a[220:223], a[56:59], v[104:107]\n\t"
    "v_mul_f32_e32 v245, v245, v165\n\t"
    "v_mul_f32_e32 v246, v246, v166\n\t"
    "v_mfma_f32_16x16x32_bf16 v[108:111], a[220:223], a[60:63], v[108:111]\n\t"
    "v_mul_f32_e32 v247, v247, v167\n\t"
    "v_mul_f32_e32 v248, v248, v180\n\t"
    "v_mfma_f32_16x16x32_bf16 v[92:95], a[216:219], a[60:63], v[92:95]\n\t"
    "v_mul_f32_e32 v249, v249, v181\n\t"
    "v_mul_f32_e32 v250, v250, v182\n\t"
    "s_waitcnt vmcnt(8)\n\t"
    "v_mfma_f32_16x16x32_bf16 v[80:83], a[224:227], a[64:67], v[80:83]\n\t"
    "buffer_load_dwordx4 a[216:219], v1, %[sW], s40 offen offset:1024 sc1\n\t"
    "v_mul_f32_e32 v251, v251, v183\n\t"
    "v_mfma_f32_16x16x32_bf16 v[96:99], a[228:231], a[64:67], v[96:99]\n\t"
    "v_cvt_pk_bf16_f32 v244, v244, v248\n\t"
    "v_cvt_pk_bf16_f32 v245, v245, v249\n\t"
    "v_mfma_f32_16x16x32_bf16 v[100:103], a[228:231], a[68:71], v[100:103]\n\t"
    "v_cvt_pk_bf16_f32 v246, v246, v250\n\t"
    "v_cvt_pk_bf16_f32 v247, v247, v251\n\t"
    "v_mfma_f32_16x16x32_bf16 v[84:87], a[224:227], a[68:71], v[84:87]\n\t"
    "ds_write_b32 v0, v244 offset:1028\n\t"
    "ds_write_b32 v3, v245 offset:1024\n\t"
    "v_mfma_f32_16x16x32_bf16 v[88:91], a[224:227], a[72:75], v[88:91]\n\t"
    "buffer_load_dwordx4 a[220:223], v1, %[sW], s40 offen offset:2048 sc1\n\t"
    "ds_write_b32 v3, v246 offset:1036\n\t"
    "v_mfma_f32_16x16x32_bf16 v[104:107], a[228:231], a[72:75], v[104:107]\n\t"
    "ds_write_b32 v4, v247 offset:1032\n\t"
    "v_add_f32_e32 v240, v56, v232\n\t"
    "v_mfma_f32_16x16x32_bf16 v[108:111], a[228:231], a[76:79], v[108:111]\n\t"
    "v_add_f32_e32 v241, v57, v233\n\t"
    "v_add_f32_e32 v242, v58, v234\n\t"
    "v_mfma_f32_16x16x32_bf16 v[92:95], a[224:227], a[76:79], v[92:95]\n\t"
    "v_add_f32_e32 v243, v59, v235\n\t"
    "v_add_f32_e32 v248, v72, v236\n\t"
    "s_waitcnt vmcnt(8)\n\t"
    "v_mfma_f32_16x16x32_bf16 v[80:83], a[232:235], a[96:99], v[80:83]\n\t"
    "buffer_load_dwordx4 a[224:227], v1, %[sW], s40 offen offset:3072 sc1\n\t"
    "s_add_u32 s40, s40, 4096\n\t"
    "v_mfma_f32_16x16x32_bf16 v[96:99], a[236:239], a[96:99], v[96:99]\n\t"
    "v_add_f32_e32 v249, v73, v237\n\t"
    "v_add_f32_e32 v250, v74, v238\n\t"
    "v_mfma_f32_16x16x32_bf16 v[100:103], a[236:239], a[100:103], v[100:103]\n\t"
    "v_add_f32_e32 v251, v75, v239\n\t"
    "v_mul_f32_e32 v240, v240, v168\n\t"
    "v_mfma_f32_16x16x32_bf16 v[84:87], a[232:235], a[100:103], v[84:87]\n\t"
    "v_mul_f32_e32 v241, v241, v169\n\t"
    "v_mul_f32_e32 v242, v242, v170\n\t"
    "v_mfma_f32_16x16x32_bf16 v[88:91], a[232:235], a[104:107], v[88:91]\n\t"
    "buffer_load_dwordx4 a[228:231], v1, %[sW], s40 offen offset:0 sc1\n\t"
    "v_mul_f32_e32 v243, v243, v171\n\t"
    "v_mfma_f32_16x16x32_bf16 v[104:107], a[236:239], a[104:107], v[104:107]\n\t"
    "v_mul_f32_e32 v248, v248, v184\n\t"
    "v_mul_f32_e32 v249, v249, v185\n\t"
    "v_mfma_f32_16x16x32_bf16 v[108:111], a[236:239], a[108:111], v[108:111]\n\t"
    "v_mul_f32_e32 v250, v250, v186\n\t"
    "v_mul_f32_e32 v251, v251, v187\n\t"
    "v_mfma_f32_16x16x32_bf16 v[92:95], a[232:235], a[108:111], v[92:95]\n\t"
    "v_cvt_pk_bf16_f32 v240, v240, v248\n\t"
    "v_cvt_pk_bf16_f32 v241, v241, v249\n\t"
    "s_waitcnt vmcnt(8)\n\t"
    "v_mfma_f32_16x16x32_bf16 v[80:83], a[240:243], a[112:115], v[80:83]\n\t"
    "buffer_load_dwordx4 a[232:235], v1, %[sW], s40 offen offset:1024 sc1\n\t"
    "v_cvt_pk_bf16_f32 v242, v242, v250\n\t"
    "v_mfma_f32_16x16x32_bf16 v[96:99], a[248:251], a[112:115], v[96:99]\n\t"
    "v_cvt_pk_bf16_f32 v243, v243, v251\n\t"
    "ds_write_b32 v0, v240 offset:2052\n\t"
    "v_mfma_f32_16x16x32_bf16 v[100:103], a[248:251], a[116:119], v[100:103]\n\t"
    "ds_write_b32 v3, v241 offset:2048\n\t"
    "ds_write_b32 v3, v242 offset:2060\n\t"
    "v_mfma_f32_16x16x32_bf16 v[84:87], a[240:243], a[116:119], v[84:87]\n\t"
    "ds_write_b32 v4, v243 offset:2056\n\t"
    "v_add_f32_e32 v244, v60, v232\n\t"
    "v_mfma_f32_16x16x32_bf16 v[88:91], a[240:243], a[120:123], v[88:91]\n\t"
    "buffer_load_dwordx4 a[236:239], v1, %[sW], s40 offen offset:2048 sc1\n\t"
    "v_add_f32_e32 v245, v61, v233\n\t"
    "v_mfma_f32_16x16x32_bf16 v[104:107], a[248:251], a[120:123], v[104:107]\n\t"
    "v_add_f32_e32 v246, v62, v234\n\t"
    "v_add_f32_e32 v247, v63, v235\n\t"
    "v_mfma_f32_16x16x32_bf16 v[108:111], a[248:251], a[124:127], v[108:111]\n\t"
    "v_add_f32_e32 v248, v76, v236\n\t"
    "v_add_f32_e32 v249, v77, v237\n\t"
    "v_mfma_f32_16x16x32_bf16 v[92:95], a[240:243], a[124:127], v[92:95]\n\t"
    "v_add_f32_e32 v250, v78, v238\n\t"
    "v_add_f32_e32 v251, v79, v239\n\t"
    "s_waitcnt vmcnt(8)\n\t"
    "v_mfma_f32_16x16x32_bf16 v[80:83], a[252:255], a[144:147], v[80:83]\n\t"
    "buffer_load_dwordx4 a[240:243], v1, %[sW], s40 offen offset:3072 sc1\n\t"
    "s_add_u32 s40, s40, 4096\n\t"
    "v_mfma_f32_16x16x32_bf16 v[96:99], v[252:255], a[144:147], v[96:99]\n\t"
    "v_mul_f32_e32 v244, v244, v172\n\t"
    "v_mul_f32_e32 v245, v245, v173\n\t"
    "v_mfma_f32_16x16x32_bf16 v[100:103], v[252:255], a[148:151], v[100:103]\n\t"
    "v_mul_f32_e32 v246, v246, v174\n\t"
    "v_mul_f32_e32 v247, v247, v175\n\t"
    "v_mfma_f32_16x16x32_bf16 v[84:87], a[252:255], a[148:151], v[84:87]\n\t"
    "v_mul_f32_e32 v248, v248, v188\n\t"
    "v_mul_f32_e32 v249, v249, v189\n\t"
    "v_mfma_f32_16x16x32_bf16 v[88:91], a[252:255], a[152:155], v[88:91]\n\t"
    "buffer_load_dwordx4 a[244:247], v1, %[sW], s40 offen offset:0 sc1\n\t"
    "v_mul_f32_e32 v250, v250, v190\n\t"
    "v_mfma_f32_16x16x32_bf16 v[104:107], v[252:255], a[152:155], v[104:107]\n\t"
    "v_mul_f32_e32 v251, v251, v191\n\t"
    "v_cvt_pk_bf16_f32 v244, v244, v248\n\t"
    "v_mfma_f32_16x16x32_bf16 v[108:111], v[252:255], a[156:159], v[108:111]\n\t"
    "buffer_load_dwordx4 a[248:251], v1, %[sW], s40 offen offset:1024 sc1\n\t"
    "v_cvt_pk_bf16_f32 v245, v245, v249\n\t"
    "v_mfma_f32_16x16x32_bf16 v[92:95], a[252:255], a[156:159], v[92:95]\n\t"
    "v_cvt_pk_bf16_f32 v246, v246, v250\n\t"
    "v_cvt_pk_bf16_f32 v247, v247, v251\n\t"
    "s_waitcnt vmcnt(9)\n\t"
    "v_mfma_f32_16x16x32_bf16 v[80:83], a[208:211], a[160:163], v[80:83]\n\t"
    "buffer_load_dwordx4 v[252:255], v1, %[sW], s40 offen offset:2048 sc1\n\t"
    "ds_write_b32 v0, v244 offset:3076\n\t"
    "v_mfma_f32_16x16x32_bf16 v[96:99], a[212:215], a[160:163], v[96:99]\n\t"
    "ds_write_b32 v3, v245 offset:3072\n\t"
    "ds_write_b32 v3, v246 offset:3084\n\t"
    "v_mfma_f32_16x16x32_bf16 v[100:103], a[212:215], a[164:167], v[100:103]\n\t"
    "ds_write_b32 v4, v247 offset:3080\n\t"
    "ds_read_b128 v[232:235], v10 offset:384\n\t"
    "v_mfma_f32_16x16x32_bf16 v[84:87], a[208:211], a[164:167], v[84:87]\n\t"
    "ds_read_b128 v[236:239], v10 offset:320\n\t"
    "s_waitcnt lgkmcnt(0)\n\t"
    "v_mfma_f32_16x16x32_bf16 v[88:91], a[208:211], a[168:171], v[88:91]\n\t"
    "s_barrier\n\t"
    "ds_read_b128 v[248:251], v8 offset:0\n\t"
    "v_mfma_f32_16x16x32_bf16 v[104:107], a[212:215], a[168:171], v[104:107]\n\t"
    "ds_read_b128 v[12:15], v8 offset:4096\n\t"
    "s_nop 0\n\t"
    "v_mfma_f32_16x16x32_bf16 v[108:111], a[212:215], a[172:175], v[108:111]\n\t"
    "s_nop 0\n\t"
    "s_nop 0\n\t"
    "v_mfma_f32_16x16x32_bf16 v[92:95], a[208:211], a[172:175], v[92:95]\n\t"
    "s_nop 0\n\t"
    "s_nop 0\n\t"
    "s_waitcnt vmcnt(8)\n\t"
    "v_mfma_f32_16x16x32_bf16 v[80:83], a[216:219], a[192:195], v[80:83]\n\t"
    "s_nop 0\n\t"
    "s_waitcnt lgkmcnt(0)\n\t"
    "v_add_f32_e32 v240, v248, v12\n\t"
    "v_mfma_f32_16x16x32_bf16 v[96:99], a[220:223], a[192:195], v[96:99]\n\t"
    "v_add_f32_e32 v241, v249, v13\n\t"
    "v_add_f32_e32 v242, v250, v14\n\t"
    "v_mfma_f32_16x16x32_bf16 v[100:103], a[220:223], a[196:199], v[100:103]\n\t"
    "v_add_f32_e32 v243, v251, v15\n\t"
    "v_add_f32_e32 v240, v240, v232\n\t"
    "v_mfma_f32_16x16x32_bf16 v[84:87], a[216:219], a[196:199], v[84:87]\n\t"
    "v_add_f32_e32 v241, v241, v233\n\t"
    "v_add_f32_e32 v242, v242, v234\n\t"
    "v_mfma_f32_16x16x32_bf16 v[88:91], a[216:219], a[200:203], v[88:91]\n\t"
    "v_add_f32_e32 v243, v243, v235\n\t"
    "v_mul_f32_e32 v240, v240, v224\n\t"
    "v_mfma_f32_16x16x32_bf16 v[104:107], a[220:223], a[200:203], v[104:107]\n\t"
    "v_mul_f32_e32 v241, v241, v225\n\t"
    "v_mul_f32_e32 v242, v242, v226\n\t"
    "v_mfma_f32_16x16x32_bf16 v[108:111], a[220:223], a[204:207], v[108:111]\n\t"
    "v_mul_f32_e32 v243, v243, v227\n\t"
    "ds_read_b128 v[232:235], v10 offset:448\n\t"
    "v_mfma_f32_16x16x32_bf16 v[92:95], a[216:219], a[204:207], v[92:95]\n\t"
    "ds_read_b128 v[248:251], v8 offset:8192\n\t"
    "ds_read_b128 v[12:15], v8 offset:12288\n\t"
    "s_waitcnt vmcnt(6)\n\t"
    "v_mfma_f32_16x16x32_bf16 v[80:83], a[224:227], a[32:35], v[80:83]\n\t"
    "s_nop 0\n\t"
    "s_nop 0\n\t"
    "v_mfma_f32_16x16x32_bf16 v[96:99], a[228:231], a[32:35], v[96:99]\n\t"
    "s_nop 0\n\t"
    "s_nop 0\n\t"
    "v_mfma_f32_16x16x32_bf16 v[100:103], a[228:231], a[36:39], v[100:103]\n\t"
    "s_nop 0\n\t"
    "s_nop 0\n\t"
    "v_mfma_f32_16x16x32_bf16 v[84:87], a[224:227], a[36:39], v[84:87]\n\t"
    "s_waitcnt lgkmcnt(0)\n\t"
    "v_add_f32_e32 v248, v248, v12\n\t"
    "v_add_f32_e32 v249, v249, v13\n\t"
    "v_mfma_f32_16x16x32_bf16 v[88:91], a[224:227], a[40:43], v[88:91]\n\t"
    "v_add_f32_e32 v250, v250, v14\n\t"
    "v_add_f32_e32 v251, v251, v15\n\t"
    "v_mfma_f32_16x16x32_bf16 v[104:107], a[228:231], a[40:43], v[104:107]\n\t"
    "v_add_f32_e32 v248, v248, v232\n\t"
    "v_add_f32_e32 v249, v249, v233\n\t"
    "v_mfma_f32_16x16x32_bf16 v[108:111], a[228:231], a[44:47], v[108:111]\n\t"
    "v_add_f32_e32 v250, v250, v234\n\t"
    "v_add_f32_e32 v251, v251, v235\n\t"
    "v_mfma_f32_16x16x32_bf16 v[92:95], a[224:227], a[44:47], v[92:95]\n\t"
    "v_mul_f32_e32 v248, v248, v228\n\t"
    "v_mul_f32_e32 v249, v249, v229\n\t"
    "s_waitcnt vmcnt(4)\n\t"
    "v_mfma_f32_16x16x32_bf16 v[80:83], a[232:235], a[80:83], v[80:83]\n\t"
    "v_mul_f32_e32 v250, v250, v230\n\t"
    "v_mul_f32_e32 v251, v251, v231\n\t"
    "v_mfma_f32_16x16x32_bf16 v[96:99], a[236:239], a[80:83], v[96:99]\n\t"
    "v_cvt_pk_bf16_f32 v240, v240, v248\n\t"
    "v_cvt_pk_bf16_f32 v241, v241, v249\n\t"
    "v_mfma_f32_16x16x32_bf16 v[100:103], a[236:239], a[84:87], v[100:103]\n\t"
    "v_cvt_pk_bf16_f32 v242, v242, v250\n\t"
    "v_cvt_pk_bf16_f32 v243, v243, v251\n\t"
    "v_mfma_f32_16x16x32_bf16 v[84:87], a[232:235], a[84:87], v[84:87]\n\t"
    "ds_write_b128 v9, v[240:243] offset:0\n\t"
    "ds_read_b128 v[232:235], v10 offset:256\n\t"
    "v_mfma_f32_16x16x32_bf16 v[88:91], a[232:235], a[88:91], v[88:91]\n\t"
    "v_mfma_f32_16x16x32_bf16 v[104:107], a[236:239], a[88:91], v[104:107]\n\t"
    "v_mfma_f32_16x16x32_bf16 v[108:111], a[236:239], a[92:95], v[108:111]\n\t"
    "v_mfma_f32_16x16x32_bf16 v[92:95], a[232:235], a[92:95], v[92:95]\n\t"
    "s_waitcnt vmcnt(2)\n\t"
    "v_mfma_f32_16x16x32_bf16 v[80:83], a[240:243], a[128:131], v[80:83]\n\t"
    "v_mfma_f32_16x16x32_bf16 v[96:99], a[244:247], a[128:131], v[96:99]\n\t"
    "v_mfma_f32_16x16x32_bf16 v[100:103], a[244:247], a[132:135], v[100:103]\n\t"
    "v_mfma_f32_16x16x32_bf16 v[84:87], a[240:243], a[132:135], v[84:87]\n\t"
    "v_mfma_f32_16x16x32_bf16 v[88:91], a[240:243], a[136:139], v[88:91]\n\t"
    "v_mfma_f32_16x16x32_bf16 v[104:107], a[244:247], a[136:139], v[104:107]\n\t"
    "v_mfma_f32_16x16x32_bf16 v[108:111], a[244:247], a[140:143], v[108:111]\n\t"
    "v_mfma_f32_16x16x32_bf16 v[92:95], a[240:243], a[140:143], v[92:95]\n\t"
    "s_waitcnt vmcnt(0)\n\t"
    "v_mfma_f32_16x16x32_bf16 v[80:83], a[248:251], a[176:179], v[80:83]\n\t"
    "v_mfma_f32_16x16x32_bf16 v[96:99], v[252:255], a[176:179], v[96:99]\n\t"
    "v_mfma_f32_16x16x32_bf16 v[100:103], v[252:255], a[180:183], v[100:103]\n\t"
    "v_mfma_f32_16x16x32_bf16 v[84:87], a[248:251], a[180:183], v[84:87]\n\t"
    "v_mfma_f32_16x16x32_bf16 v[88:91], a[248:251], a[184:187], v[88:91]\n\t"
    "v_mfma_f32_16x16x32_bf16 v[104:107], v[252:255], a[184:187], v[104:107]\n\t"
    "v_mfma_f32_16x16x32_bf16 v[108:111], v[252:255], a[188:191], v[108:111]\n\t"
    "v_mfma_f32_16x16x32_bf16 v[92:95], a[248:251], a[188:191], v[92:95]\n\t"
    "v_mbcnt_lo_u32_b32 v128, -1, 0\n\t"
    "v_mbcnt_hi_u32_b32 v128, -1, v128\n\t"
    "v_bfe_u32 v129, v128, 2, 3\n\t"
    "v_and_b32_e32 v130, 3, v128\n\t"
    "v_lshrrev_b32_e32 v131, 5, v128\n\t"
    "v_mul_u32_u24_e32 v132, 1664, v129\n\t"
    "v_lshl_add_u32 v132, v130, 4, v132\n\t"
    "v_lshl_add_u32 v132, v131, 6, v132\n\t"
    "v_and_b32_e32 v136, 1, v130\n\t"
    "v_lshlrev_b32_e32 v136, 4, v136\n\t"
    "v_and_b32_e32 v137, 2, v130\n\t"
    "v_lshl_or_b32 v136, v137, 5, v136\n\t"
    "v_lshlrev_b32_e32 v133, 5, v129\n\t"
    "v_xor_b32_e32 v133, v133, v136\n\t"
    "v_lshl_add_u32 v133, v130, 8, v133\n\t"
    "v_xor_b32_e32 v134, 16, v133\n\t"
    "s_lshl_b32 s42, %[wv], 10\n\t"
    "s_add_u32 s42, s42, %[lds]\n\t"
    "s_add_u32 s42, s42, 82176\n\t"
    "v_add_u32_e32 v133, s42, v133\n\t"
    "v_add_u32_e32 v134, s42, v134\n\t"
    "v_add_u32_e32 v135, 49152, v133\n\t"
    "v_cmp_eq_u32_e32 vcc, 0, v131\n\t"
    "v_lshl_add_u32 v133, v131, 12, v133\n\t"
    "s_mul_i32 s42, %[wv], 13312\n\t"
    "v_cndmask_b32_e32 v135, v134, v135, vcc\n\t"
    "v_lshl_add_u32 v134, v131, 12, v134\n\t"
    "v_add_u32_e32 v134, 4096, v134\n\t"
    "s_add_u32 s45, s46, s42\n\t"
    "s_add_u32 s42, %[lds], 81920\n\t"
    "v_mov_b32_e32 v12, s42\n\t"
    "ds_read_b32 v12, v12\n\t"
    "s_waitcnt lgkmcnt(1)\n\t"
    "v_add_f32_e32 v240, v80, v232\n\t"
    "v_add_f32_e32 v241, v81, v233\n\t"
    "v_add_f32_e32 v242, v82, v234\n\t"
    "v_add_f32_e32 v243, v83, v235\n\t"
    "v_add_f32_e32 v248, v96, v236\n\t"
    "v_add_f32_e32 v249, v97, v237\n\t"
    "v_add_f32_e32 v250, v98, v238\n\t"
    "v_add_f32_e32 v251, v99, v239\n\t"
    "v_mul_f32_e32 v240, v240, v192\n\t"
    "v_mul_f32_e32 v241, v241, v193\n\t"
    "v_mul_f32_e32 v242, v242, v194\n\t"
    "v_mul_f32_e32 v243, v243, v195\n\t"
    "v_mul_f32_e32 v248, v248, v208\n\t"
    "v_mul_f32_e32 v249, v249, v209\n\t"
    "v_mul_f32_e32 v250, v250, v210\n\t"
    "v_mul_f32_e32 v251, v251, v211\n\t"
    "v_cvt_pk_bf16_f32 v240, v240, v248\n\t"
    "v_cvt_pk_bf16_f32 v241, v241, v249\n\t"
    "v_cvt_pk_bf16_f32 v242, v242, v250\n\t"
    "v_cvt_pk_bf16_f32 v243, v243, v251\n\t"
    "ds_write_b32 v0, v240 offset:8\n\t"
    "ds_write_b32 v3, v241 offset:4\n\t"
    "ds_write_b32 v4, v242 offset:0\n\t"
    "ds_write_b32 v4, v243 offset:12\n\t"
    "v_add_f32_e32 v244, v84, v232\n\t"
    "v_add_f32_e32 v245, v85, v233\n\t"
    "v_add_f32_e32 v246, v86, v234\n\t"
    "v_add_f32_e32 v247, v87, v235\n\t"
    "v_add_f32_e32 v248, v100, v236\n\t"
    "v_add_f32_e32 v249, v101, v237\n\t"
    "v_add_f32_e32 v250, v102, v238\n\t"
    "v_add_f32_e32 v251, v103, v239\n\t"
    "v_mul_f32_e32 v244, v244, v196\n\t"
    "v_mul_f32_e32 v245, v245, v197\n\t"
    "v_mul_f32_e32 v246, v246, v198\n\t"
    "v_mul_f32_e32 v247, v247, v199\n\t"
    "v_mul_f32_e32 v248, v248, v212\n\t"
    "v_mul_f32_e32 v249, v249, v213\n\t"
    "v_mul_f32_e32 v250, v250, v214\n\t"
    "v_mul_f32_e32 v251, v251, v215\n\t"
    "v_cvt_pk_bf16_f32 v244, v244, v248\n\t"
    "v_cvt_pk_bf16_f32 v245, v245, v249\n\t"
    "v_cvt_pk_bf16_f32 v246, v246, v250\n\t"
    "v_cvt_pk_bf16_f32 v247, v247, v251\n\t"
    "ds_write_b32 v0, v244 offset:1032\n\t"
    "ds_write_b32 v3, v245 offset:1028\n\t"
    "ds_write_b32 v4, v246 offset:1024\n\t"
    "ds_write_b32 v4, v247 offset:1036\n\t"
    "v_add_f32_e32 v240, v88, v232\n\t"
    "v_add_f32_e32 v241, v89, v233\n\t"
    "v_add_f32_e32 v242, v90, v234\n\t"
    "v_add_f32_e32 v243, v91, v235\n\t"
    "v_add_f32_e32 v248, v104, v236\n\t"
    "v_add_f32_e32 v249, v105, v237\n\t"
    "v_add_f32_e32 v250, v106, v238\n\t"
    "v_add_f32_e32 v251, v107, v239\n\t"
    "v_mul_f32_e32 v240, v240, v200\n\t"
    "v_mul_f32_e32 v241, v241, v201\n\t"
    "v_mul_f32_e32 v242, v242, v202\n\t"
    "v_mul_f32_e32 v243, v243, v203\n\t"
    "v_mul_f32_e32 v248, v248, v216\n\t"
    "v_mul_f32_e32 v249, v249, v217\n\t"
    "v_mul_f32_e32 v250, v250, v218\n\t"
    "v_mul_f32_e32 v251, v251, v219\n\t"
    "v_cvt_pk_bf16_f32 v240, v240, v248\n\t"
    "v_cvt_pk_bf16_f32 v241, v241, v249\n\t"
    "v_cvt_pk_bf16_f32 v242, v242, v250\n\t"
    "v_cvt_pk_bf16_f32 v243, v243, v251\n\t"
    "ds_write_b32 v0, v240 offset:2056\n\t"
    "ds_write_b32 v3, v241 offset:2052\n\t"
    "ds_write_b32 v4, v242 offset:2048\n\t"
    "ds_write_b32 v4, v243 offset:2060\n\t"
    "v_add_f32_e32 v244, v92, v232\n\t"
    "v_add_f32_e32 v245, v93, v233\n\t"
    "v_add_f32_e32 v246, v94, v234\n\t"
    "v_add_f32_e32 v247, v95, v235\n\t"
    "v_add_f32_e32 v248, v108, v236\n\t"
    "v_add_f32_e32 v249, v109, v237\n\t"
    "v_add_f32_e32 v250, v110, v238\n\t"
    "v_add_f32_e32 v251, v111, v239\n\t"
    "v_mul_f32_e32 v244, v244, v204\n\t"
    "v_mul_f32_e32 v245, v245, v205\n\t"
    "v_mul_f32_e32 v246, v246, v206\n\t"
    "v_mul_f32_e32 v247, v247, v207\n\t"
    "v_mul_f32_e32 v248, v248, v220\n\t"
    "v_mul_f32_e32 v249, v249, v221\n\t"
    "v_mul_f32_e32 v250, v250, v222\n\t"
    "v_mul_f32_e32 v251, v251, v223\n\t"
    "v_cvt_pk_bf16_f32 v244, v244, v248\n\t"
    "v_cvt_pk_bf16_f32 v245, v245, v249\n\t"
    "v_cvt_pk_bf16_f32 v246, v246, v250\n\t"
    "v_cvt_pk_bf16_f32 v247, v247, v251\n\t"
    "ds_write_b32 v0, v244 offset:3080\n\t"
    "ds_write_b32 v3, v245 offset:3076\n\t"
    "ds_write_b32 v4, v246 offset:3072\n\t"
    "ds_write_b32 v4, v247 offset:3084\n\t"
    "s_waitcnt lgkmcnt(0)\n\t"
    "s_barrier\n\t"
    "ds_read_b128 a[0:3], v133 offset:0\n\t"
    "ds_read_b128 a[4:7], v133 offset:8192\n\t"
    "ds_read_b128 a[8:11], v133 offset:16384\n\t"
    "ds_read_b128 a[12:15], v133 offset:24576\n\t"
    "ds_read_b128 a[16:19], v133 offset:32768\n\t"
    "ds_read_b128 a[20:23], v133 offset:40960\n\t"
    "ds_read_b128 a[24:27], v135 offset:0\n\t"
    "ds_read_b128 a[28:31], v134 offset:0\n\t"
    "ds_read_b128 a[32:35], v134 offset:8192\n\t"
    "ds_read_b128 a[36:39], v134 offset:16384\n\t"
    "ds_read_b128 a[40:43], v134 offset:24576\n\t"
    "ds_read_b128 a[44:47], v134 offset:32768\n\t"
    "ds_read_b128 a[48:51], v134 offset:40960\n\t"
    "v_readfirstlane_b32 %[stat], v12\n\t"
    "s_waitcnt lgkmcnt(12)\n\t"
    "buffer_store_dwordx4 a[0:3], v132, %[sO], s45 offen offset:0 sc1\n\t"
    "s_waitcnt lgkmcnt(11)\n\t"
    "buffer_store_dwordx4 a[4:7], v132, %[sO], s45 offen offset:128 sc1\n\t"
    "s_waitcnt lgkmcnt(10)\n\t"
    "buffer_store_dwordx4 a[8:11], v132, %[sO], s45 offen offset:256 sc1\n\t"
    "s_waitcnt lgkmcnt(9)\n\t"
    "buffer_store_dwordx4 a[12:15], v132, %[sO], s45 offen offset:384 sc1\n\t"
    "s_waitcnt lgkmcnt(8)\n\t"
    "buffer_store_dwordx4 a[16:19], v132, %[sO], s45 offen offset:512 sc1\n\t"
    "s_waitcnt lgkmcnt(7)\n\t"
    "buffer_store_dwordx4 a[20:23], v132, %[sO], s45 offen offset:640 sc1\n\t"
    "s_waitcnt lgkmcnt(6)\n\t"
    "buffer_store_dwordx4 a[24:27], v132, %[sO], s45 offen offset:768 sc1\n\t"
    "s_waitcnt lgkmcnt(5)\n\t"
    "buffer_store_dwordx4 a[28:31], v132, %[sO], s45 offen offset:896 sc1\n\t"
    "s_waitcnt lgkmcnt(4)\n\t"
    "buffer_store_dwordx4 a[32:35], v132, %[sO], s45 offen offset:1024 sc1\n\t"
    "s_waitcnt lgkmcnt(3)\n\t"
    "buffer_store_dwordx4 a[36:39], v132, %[sO], s45 offen offset:1152 sc1\n\t"
    "s_waitcnt lgkmcnt(2)\n\t"
    "buffer_store_dwordx4 a[40:43], v132, %[sO], s45 offen offset:1280 sc1\n\t"
    "s_waitcnt lgkmcnt(1)\n\t"
    "buffer_store_dwordx4 a[44:47], v132, %[sO], s45 offen offset:1408 sc1\n\t"
    "s_waitcnt lgkmcnt(0)\n\t"
    "buffer_store_dwordx4 a[48:51], v132, %[sO], s45 offen offset:1536 sc1\n\t"
    "s_cmp_lg_u32 %[stat], 0\n\t"
    "s_cbranch_scc1 98f\n\t"
    "s_cmp_lg_u32 %[wv], 3\n\t"
    "s_cbranch_scc1 96f\n\t"
    "s_sub_u32 s42, s51, 1\n\t"
    "v_mov_b32_e32 v12, s42\n\t"
    "v_writelane_b32 v12, s51, 2\n\t"
    "s_mul_i32 s42, %[blk], 128\n\t"
    "s_add_u32 s42, s42, 2088960\n\t"
    "v_mov_b32_e32 v13, s42\n\t"
    "s_lshr_b32 s42, %[blk], 3\n\t"
    "s_lshl_b32 s42, s42, 7\n\t"
    "s_and_b32 s45, %[blk], 7\n\t"
    "s_lshl_b32 s45, s45, 2\n\t"
    "s_add_u32 s42, s42, s45\n\t"
    "s_add_u32 s42, s42, 2121856\n\t"
    "v_writelane_b32 v13, s42, 1\n\t"
    "s_mov_b32 s42, 2121728\n\t"
    "v_writelane_b32 v13, s42, 2\n\t"
    "s_cmp_eq_u32 %[blk], 0\n\t"
    "s_cselect_b64 exec, 7, 3\n\t"
    "buffer_store_dword v12, v13, %[sW], 0 offen sc1\n\t"
    "s_mov_b64 exec, -1\n\t"
    "96:\n\t"
    "s_endpgm\n\t"
    "s_branch 99f\n\t"
    "62:\n\t"
    "s_sub_u32 s45, s45, 1\n\t"
    "s_cmp_eq_u32 s45, 0\n\t"
    "s_cbranch_scc1 65f\n\t"
    "s_sleep 8\n\t"
    "v_mbcnt_lo_u32_b32 v12, -1, 0\n\t"
    "v_mbcnt_hi_u32_b32 v12, -1, v12\n\t"
    "v_and_b32_e32 v13, 1, v12\n\t"
    "v_lshrrev_b32_e32 v12, 1, v12\n\t"
    "v_lshlrev_b32_e32 v12, 7, v12\n\t"
    "v_lshl_or_b32 v12, v13, 4, v12\n\t"
    "s_mov_b32 s42, 2121856\n\t"
    "buffer_load_dwordx4 v[12:15], v12, %[sW], s42 offen sc1\n\t"
    "s_waitcnt vmcnt(0)\n\t"
    "s_branch 64b\n\t"
    "65:\n\t"
    "s_add_u32 s42, %[lds], 81920\n\t"
    "v_mov_b32_e32 v12, s42\n\t"
    "v_mov_b32_e32 v13, 1\n\t"
    "ds_write_b32 v12, v13\n\t"
    "s_waitcnt lgkmcnt(0)\n\t"
    "s_branch 63b\n\t"
    "98:\n\t"
    "s_waitcnt vmcnt(0) lgkmcnt(0)\n\t"
    "99:\n\t"
    ".Ldcn_hot_end:\n\t"
    : [tid] "+v"(tid), [stat] "=&s"(stat), [tag] "=&s"(tag)
    : [sX] "s"(sX), [sS] "s"(sS), [aS] "s"(ua.src), [aG] "s"(ua.gs), [aR] "s"(ua.rs), [aD0] "s"(ua.d0), [aD1] "s"(ua.d1), [aD2] "s"(ua.d2),
      [bS] "s"(ub.src), [bG] "s"(ub.gs), [bC] "s"(ub.cs), [bR] "s"(ub.rs), [bD0] "s"(ub.d0), [bD1] "s"(ub.d1), [bZ] "s"(bz),
      [sO] "s"(sO), [sW] "s"(sW), [sB] "s"(sB), [wv] "s"(wv), [blk] "s"(blk), [lds] "s"(lds)
    : "v1", "v2", "v3", "v4", "v5", "v6", "v7", "v8", "v9", "v10", "v11", "v12", "v13", "v14", "v15", "v16", "v17", "v18", "v19", "v20", "v21", "v22", "v23", "v24", "v25", "v26", "v27", "v28", "v29", "v30", "v31", "v32", "v33", "v34", "v35", "v36", "v37", "v38", "v39", "v40", "v41", "v42", "v43", "v44", "v45", "v46", "v47", "v48", "v49", "v50", "v51", "v52", "v53", "v54", "v55", "v56", "v57", "v58", "v59", "v60", "v61", "v62", "v63", "v64", "v65", "v66", "v67", "v68", "v69", "v70", "v71", "v72", "v73", "v74", "v75", "v76", "v77", "v78", "v79", "v80", "v81", "v82", "v83", "v84", "v85", "v86", "v87", "v88", "v89", "v90", "v91", "v92", "v93", "v94", "v95", "v96", "v97", "v98", "v99", "v100", "v101", "v102", "v103", "v104", "v105", "v106", "v107", "v108", "v109", "v110", "v111", "v112", "v113", "v114", "v115", "v116", "v117", "v118", "v119", "v120", "v121", "v122", "v123", "v124", "v125", "v126", "v127", "v128", "v129", "v130", "v131", "v132", "v133", "v134", "v135", "v136", "v137", "v138", "v139", "v140", "v141", "v142", "v143", "v144", "v145", "v146", "v147", "v148", "v149", "v150", "v151", "v152", "v153", "v154", "v155", "v156", "v157", "v158", "v159", "v160", "v161", "v162", "v163", "v164", "v165", "v166", "v167", "v168", "v169", "v170", "v171", "v172", "v173", "v174", "v175", "v176", "v177", "v178", "v179", "v180", "v181", "v182", "v183", "v184", "v185", "v186", "v187", "v188", "v189", "v190", "v191", "v192", "v193", "v194", "v195", "v196", "v197", "v198", "v199", "v200", "v201", "v202", "v203", "v204", "v205", "v206", "v207", "v208", "v209", "v210", "v211", "v212", "v213", "v214", "v215", "v216", "v217", "v218", "v219", "v220", "v221", "v222", "v223", "v224", "v225", "v226", "v227", "v228", "v229", "v230", "v231", "v232", "v233", "v234", "v235", "v236", "v237", "v238", "v239", "v240", "v241", "v242", "v243", "v244", "v245", "v246", "v247", "v248", "v249", "v250", "v251", "v252", "v253", "v254", "v255", "a0", "a1", "a2", "a3", "a4", "a5", "a6", "a7", "a8", "a9", "a10", "a11", "a12", "a13", "a14", "a15", "a16", "a17", "a18", "a19", "a20", "a21", "a22", "a23", "a24", "a25", "a26", "a27", "a28", "a29", "a30", "a31", "a32", "a33", "a34", "a35", "a36", "a37", "a38", "a39", "a40", "a41", "a42", "a43", "a44", "a45", "a46", "a47", "a48", "a49", "a50", "a51", "a52", "a53", "a54", "a55", "a56", "a57", "a58", "a59", "a60", "a61", "a62", "a63", "a64", "a65", "a66", "a67", "a68", "a69", "a70", "a71", "a72", "a73", "a74", "a75", "a76", "a77", "a78", "a79", "a80", "a81", "a82", "a83", "a84", "a85", "a86", "a87", "a88", "a89", "a90", "a91", "a92", "a93", "a94", "a95", "a96", "a97", "a98", "a99", "a100", "a101", "a102", "a103", "a104", "a105", "a106", "a107", "a108", "a109", "a110", "a111", "a112", "a113", "a114", "a115", "a116", "a117", "a118", "a119", "a120", "a121", "a122", "a123", "a124", "a125", "a126", "a127", "a128", "a129", "a130", "a131", "a132", "a133", "a134", "a135", "a136", "a137", "a138", "a139", "a140", "a141", "a142", "a143", "a144", "a145", "a146", "a147", "a148", "a149", "a150", "a151", "a152", "a153", "a154", "a155", "a156", "a157", "a158", "a159", "a160", "a161", "a162", "a163", "a164", "a165", "a166", "a167", "a168", "a169", "a170", "a171", "a172", "a173", "a174", "a175", "a176", "a177", "a178", "a179", "a180", "a181", "a182", "a183", "a184", "a185", "a186", "a187", "a188", "a189", "a190", "a191", "a192", "a193", "a194", "a195", "a196", "a197", "a198", "a199", "a200", "a201", "a202", "a203", "a204", "a205", "a206", "a207", "a208", "a209", "a210", "a211", "a212", "a213", "a214", "a215", "a216", "a217", "a218", "a219", "a220", "a221", "a222", "a223", "a224", "a225", "a226", "a227", "a228", "a229", "a230", "a231", "a232", "a233", "a234", "a235", "a236", "a237", "a238", "a239", "a240", "a241", "a242", "a243", "a244", "a245", "a246", "a247", "a248", "a249", "a250", "a251", "a252", "a253", "a254", "a255", "s40", "s41", "s42", "s43", "s44", "s45", "s46", "s47", "s48", "s49", "s50", "s51", "s52", "s53", "s54", "s55", "s56", "s57", "s58", "s59", "s60", "s61", "s62", "s63", "s64", "s65", "s66", "s67", "s68", "s69", "s70", "s71", "s72", "s73", "s74", "s75", "s76", "s77", "s78", "s79", "vcc", "scc", "memory");
  unsigned lane;
  asm volatile("v_mbcnt_lo_u32_b32 %0, -1, 0\n\tv_mbcnt_hi_u32_b32 %0, -1, %0" : "=v"(lane));
  if (stat) asm volatile("s_waitcnt vmcnt(0)" ::: "memory");
  if (stat) slow_path((const unsigned short*)x0, (const unsigned short*)w, (const unsigned short*)bs, (unsigned short*)out, (unsigned short*)smem, blk, wv * 64u + lane);
  if (stat && wv == 3u && lane == 0u) {
    __builtin_amdgcn_raw_buffer_store_b32(tag - 1u, sW, kFlagOff + 128u * blk, 0, kSc1);
    __builtin_amdgcn_raw_buffer_store_b32(tag - 1u, sW, kFlag2Off + (blk >> 3) * 128u + (blk & 7u) * 4u, 0, kSc1);
    if (blk == 0u) __builtin_amdgcn_raw_buffer_store_b32(tag, sW, kZOff, 0, kSc1);
  }
}
}

extern "C" void kernel_launch(void* const* d_in, const int* in_sizes, int n_in, void* d_out, int out_size, void* d_ws, size_t ws_size,
                              hipStream_t stream) {
  (void)n_in; (void)ws_size; (void)out_size; (void)in_sizes;
  dcn_main<<<dim3(16384 / 64), dim3(256), 0, stream>>>(d_in[0], d_in[1], d_ws, d_in[2], d_out);
}
```
